```python
import torch

def _softmax(x, axis=-1):
    return torch.softmax(x, dim=axis)
def _gelu(x):
    return torch.nn.functional.gelu(x, approximate="tanh")
def _silu(x):
    return torch.nn.functional.silu(x)
def _sigmoid(x):
    return torch.sigmoid(x)
def _layernorm(x, axis=-1, eps=1e-5):
    m = x.mean(dim=axis, keepdim=True)
    v = x.var(dim=axis, keepdim=True, unbiased=False)
    return (x - m) / torch.sqrt(v + eps)
def _scatter_add(tgt, idx, src, axis=0):
    return tgt.clone().index_add_(axis, idx, src)
def _segment_sum(src, seg, n):
    out = torch.zeros((n,) + src.shape[1:], dtype=src.dtype)
    return out.index_add_(0, seg, src)
def _pad(x, pw):
    flat = tuple(v for pair in reversed(pw) for v in pair)
    return torch.nn.functional.pad(x, flat)

kernel_name = 'standalone__deep_cross_wide_deep__recsys'

def setup_inputs():

    _w = (lambda a: (lambda t: t.to(torch.bfloat16) if t.is_floating_point() else t)(torch.as_tensor(a)))
    B, F, D = 4096, 26, 128
    V, Nd = min(1000, 100000), 13
    Din = F * D
    H = 4 * D
    return {
        "idx": _w((lambda lo, hi, *s: torch.randint(lo, hi, s))(0, V, B, F).to(torch.int64)),
        "table": _w((lambda *s: torch.randn(*s, dtype=torch.bfloat16) * 1.6)(V, D) * 0.05),
        "x_wide": _w((lambda *s: torch.randn(*s, dtype=torch.bfloat16) * 1.6)(B, Nd)),
        "w_wide": _w((lambda *s: torch.randn(*s, dtype=torch.bfloat16) * 1.6)(Nd) * 0.05),
        "b_wide": _w((lambda *s: torch.randn(*s, dtype=torch.bfloat16) * 1.6)(1) * 0.01),
        "W1": _w((lambda *s: torch.randn(*s, dtype=torch.bfloat16) * 1.6)(Din, H) * (2.0 / Din) ** 0.5),
        "b1": _w((lambda *s: torch.randn(*s, dtype=torch.bfloat16) * 1.6)(H) * 0.01),
        "W2": _w((lambda *s: torch.randn(*s, dtype=torch.bfloat16) * 1.6)(H) * (1.0 / H) ** 0.5),
        "b2": _w((lambda *s: torch.randn(*s, dtype=torch.bfloat16) * 1.6)(1) * 0.01),
    }

def reference(idx, table, x_wide, w_wide, b_wide, W1, b1, W2, b2):
    emb = table[idx]
    deep_in = emb.reshape(emb.shape[0], -1)
    h = deep_in @ W1 + b1
    h = h * (lambda x, dt: x.to(dt))(h > 0, h.dtype)
    deep_logit = h @ W2 + b2
    wide_logit = x_wide @ w_wide + b_wide
    return _sigmoid(wide_logit + deep_logit)
```

```cpp
#include <hip/hip_runtime.h>
#include <hip/hip_bf16.h>
#include <stdint.h>

typedef unsigned u32x4 __attribute__((ext_vector_type(4)));
typedef unsigned u32x2 __attribute__((ext_vector_type(2)));
typedef float f32x4 __attribute__((ext_vector_type(4)));
typedef __attribute__((address_space(1))) unsigned long long gu64;
typedef __attribute__((address_space(1))) unsigned gu32;

constexpr int B_ = 4096, F_ = 26, D_ = 128, V_ = 1000, ND_ = 13, H_ = 512;
constexpr int LDS_BYTES = 163840;
constexpr int RING0 = 131072;
constexpr int WS_GRAN = 4096;

#define ACLOB "a0","a1","a2","a3","a4","a5","a6","a7","a8","a9","a10","a11","a12","a13","a14","a15", \
  "a16","a17","a18","a19","a20","a21","a22","a23","a24","a25","a26","a27","a28","a29","a30","a31", \
  "a32","a33","a34","a35","a36","a37","a38","a39","a40","a41","a42","a43","a44","a45","a46","a47", \
  "a48","a49","a50","a51","a52","a53","a54","a55","a56","a57","a58","a59","a60","a61","a62","a63", \
  "a64","a65","a66","a67","a68","a69","a70","a71","a72","a73","a74","a75","a76","a77","a78","a79", \
  "a80","a81","a82","a83","a84","a85","a86","a87","a88","a89","a90","a91","a92","a93","a94","a95", \
  "a96","a97","a98","a99","a100","a101","a102","a103","a104","a105","a106","a107","a108","a109","a110","a111", \
  "a112","a113","a114","a115","a116","a117","a118","a119","a120","a121","a122","a123","a124","a125","a126","a127", \
  "a128","a129","a130","a131","a132","a133","a134","a135","a136","a137","a138","a139","a140","a141","a142","a143", \
  "a144","a145","a146","a147","a148","a149","a150","a151","a152","a153","a154","a155","a156","a157","a158","a159", \
  "a160","a161","a162","a163","a164","a165","a166","a167","a168","a169","a170","a171","a172","a173","a174","a175", \
  "a176","a177","a178","a179","a180","a181","a182","a183","a184","a185","a186","a187","a188","a189","a190","a191", \
  "a192","a193","a194","a195","a196","a197","a198","a199"

struct Ctx {
  unsigned addr[13][4];
  unsigned tb0, tb1;
  unsigned vW0, vW1;
  unsigned vT0, vT1, vT2, vT3;
  __amdgpu_buffer_rsrc_t srdW, srdT;
  unsigned ring;
  unsigned wbase;
  unsigned tgrp;
};

__host__ __device__ constexpr int setOf(int j) { return j == 0 ? 2 : (j & 1); }
__host__ __device__ constexpr int regE(int set, int i) { return 128 + set * 24 + i * 4; }
__host__ __device__ constexpr int regW(int set, int nb) { return 128 + set * 24 + 16 + nb * 4; }
__host__ __device__ constexpr int regC(int i, int nb) { return (i * 2 + nb) * 16; }

template <int N> __device__ __forceinline__ void waitV() { asm volatile("s_waitcnt vmcnt(%0)" ::"n"(N) : "memory"); }
template <int N> __device__ __forceinline__ void waitL() { asm volatile("s_waitcnt lgkmcnt(%0)" ::"n"(N) : "memory"); }
__device__ __forceinline__ void bar() { asm volatile("s_barrier" ::: "memory"); }

template <int R> __device__ __forceinline__ void readE(unsigned& a) {
  asm volatile("ds_read_b128 a[%c1:%c2], %0\n\tv_xor_b32 %0, 0x10000, %0" : "+v"(a) : "i"(R), "i"(R + 3) : "memory");
}
template <int R, int OFF> __device__ __forceinline__ void readW(unsigned a) {
  asm volatile("ds_read_b64_tr_b16 a[%c1:%c2], %0 offset:%c3\n\tds_read_b64_tr_b16 a[%c4:%c5], %0 offset:%c6"
               ::"v"(a), "i"(R), "i"(R + 1), "i"(OFF), "i"(R + 2), "i"(R + 3), "i"(OFF + 512) : "memory");
}
template <int C, int W, int E> __device__ __forceinline__ void mfma() {
  asm volatile("v_mfma_f32_32x32x16_bf16 a[%c0:%c1], a[%c2:%c3], a[%c4:%c5], a[%c0:%c1]"
               ::"i"(C), "i"(C + 15), "i"(W), "i"(W + 3), "i"(E), "i"(E + 3));
}
__device__ __forceinline__ void dmaW(const Ctx& c, unsigned m0v, unsigned soff) {
  asm volatile("s_mov_b32 m0, %0\n\ts_nop 0\n\tbuffer_load_dwordx4 %1, %3, %4 offen lds\n\t"
               "buffer_load_dwordx4 %2, %3, %4 offen offset:1024 lds"
               ::"s"(m0v), "v"(c.vW0), "v"(c.vW1), "s"(c.srdW), "s"(soff) : "memory");
}
template <int HALF> __device__ __forceinline__ void dmaT(const Ctx& c, unsigned m0v, unsigned soff) {
  if constexpr (HALF == 0)
    asm volatile("s_mov_b32 m0, %0\n\ts_nop 0\n\tbuffer_load_dwordx4 %1, %3, %4 offen lds\n\t"
                 "buffer_load_dwordx4 %2, %3, %4 offen offset:1024 lds"
                 ::"s"(m0v), "v"(c.vT0), "v"(c.vT1), "s"(c.srdT), "s"(soff) : "memory");
  else
    asm volatile("s_mov_b32 m0, %0\n\ts_nop 0\n\tbuffer_load_dwordx4 %1, %3, %4 offen offset:2048 lds\n\t"
                 "buffer_load_dwordx4 %2, %3, %4 offen offset:3072 lds"
                 ::"s"(m0v), "v"(c.vT2), "v"(c.vT3), "s"(c.srdT), "s"(soff) : "memory");
}

__host__ __device__ constexpr int nS(int j, bool last) { return (!last && j >= 0 && j < 8) ? 2 : 0; }
__host__ __device__ constexpr int nB(int j, bool last) { return (!last || j <= 8) ? 2 : 0; }
__host__ __device__ constexpr int opsS(int j, bool last) { return j < 0 ? nS(j + 13, false) : nS(j, last); }
__host__ __device__ constexpr int opsAll(int j, bool last) { return j < 0 ? 2 + nS(j + 13, false) : nB(j, last) + nS(j, last); }

template <int J, bool LAST> __device__ __forceinline__ void step(Ctx& c, int q, unsigned& slot) {
  constexpr int cur = setOf(J), JN = (J + 1) % 13, nxt = setOf(JN);
  constexpr bool hasNext = !(LAST && J == 12);
  const unsigned slotN = (slot + 2048) & 8191;
  if constexpr (hasNext) waitV<opsS(J - 3, LAST) + opsAll(J - 2, LAST) + opsAll(J - 1, LAST)>();
  if constexpr (J == 12 && !LAST) { waitL<0>(); bar(); }
  else if constexpr (hasNext || true) waitL<3>();
  const unsigned t0 = c.tb0 + slotN, t1 = c.tb1 + slotN;
  mfma<regC(0, 0), regW(cur, 0), regE(cur, 0)>();
  if constexpr (hasNext) readW<regW(nxt, 0), 0>(t0);
  mfma<regC(0, 1), regW(cur, 1), regE(cur, 0)>();
  if constexpr (hasNext) readW<regW(nxt, 1), 0>(t1);
  if constexpr (hasNext) waitL<6>(); else waitL<2>();
  mfma<regC(1, 0), regW(cur, 0), regE(cur, 1)>();
  if constexpr (hasNext) readE<regE(nxt, 0)>(c.addr[JN][0]);
  mfma<regC(1, 1), regW(cur, 1), regE(cur, 1)>();
  if constexpr (nB(J, LAST) != 0) {
    constexpr int J4 = (J + 4) % 13, wrap = (J + 4) >= 13 ? 1 : 0;
    dmaW(c, c.ring + slot, c.wbase + (unsigned)(q + wrap) * 32768u + (unsigned)J4 * 131072u);
  }
  if constexpr (hasNext) waitL<6>(); else waitL<1>();
  mfma<regC(2, 0), regW(cur, 0), regE(cur, 2)>();
  if constexpr (hasNext) readE<regE(nxt, 1)>(c.addr[JN][1]);
  mfma<regC(2, 1), regW(cur, 1), regE(cur, 2)>();
  if constexpr (nS(J, LAST) != 0) {
    constexpr int g = J >> 1;
    dmaT<J & 1>(c, (unsigned)(((q + 1) & 1) << 16) + c.tgrp + g * 16384u, c.tgrp * 4u + g * 65536u + (unsigned)(q + 1) * 64u);
  }
  if constexpr (hasNext) waitL<6>(); else waitL<0>();
  mfma<regC(3, 0), regW(cur, 0), regE(cur, 3)>();
  if constexpr (hasNext) readE<regE(nxt, 2)>(c.addr[JN][2]);
  mfma<regC(3, 1), regW(cur, 1), regE(cur, 3)>();
  if constexpr (hasNext) readE<regE(nxt, 3)>(c.addr[JN][3]);
  slot = slotN;
}

template <bool LAST> __device__ __forceinline__ void phase(Ctx& c, int q, unsigned& slot) {
  step<0, LAST>(c, q, slot); step<1, LAST>(c, q, slot); step<2, LAST>(c, q, slot); step<3, LAST>(c, q, slot);
  step<4, LAST>(c, q, slot); step<5, LAST>(c, q, slot); step<6, LAST>(c, q, slot); step<7, LAST>(c, q, slot);
  step<8, LAST>(c, q, slot); step<9, LAST>(c, q, slot); step<10, LAST>(c, q, slot); step<11, LAST>(c, q, slot);
  step<12, LAST>(c, q, slot);
}

template <int R> __device__ __forceinline__ void zero1() { asm volatile("v_accvgpr_write_b32 a[%c0], 0" ::"i"(R)); }
template <int R, int N> struct Zero { static __device__ __forceinline__ void run() { zero1<R>(); Zero<R + 1, N - 1>::run(); } };
template <int R> struct Zero<R, 0> { static __device__ __forceinline__ void run() {} };

template <int R, int OFF> __device__ __forceinline__ void writeAcc(unsigned a) {
  asm volatile("ds_write_b128 %0, a[%c1:%c2] offset:%c3" ::"v"(a), "i"(R), "i"(R + 3), "i"(OFF) : "memory");
}
template <int I> __device__ __forceinline__ void sendBlock(unsigned a) {
  writeAcc<I * 32 + 0, 0>(a); writeAcc<I * 32 + 4, 1024>(a); writeAcc<I * 32 + 8, 2048>(a); writeAcc<I * 32 + 12, 3072>(a);
  writeAcc<I * 32 + 16, 4096>(a); writeAcc<I * 32 + 20, 5120>(a); writeAcc<I * 32 + 24, 6144>(a); writeAcc<I * 32 + 28, 7168>(a);
}
template <int R> __device__ __forceinline__ float readAcc() {
  float v; asm volatile("v_accvgpr_read_b32 %0, a[%c1]" : "=v"(v) : "i"(R)); return v;
}
template <int G> __device__ __forceinline__ f32x4 readAcc4() {
  f32x4 v; v.x = readAcc<G * 4>(); v.y = readAcc<G * 4 + 1>(); v.z = readAcc<G * 4 + 2>(); v.w = readAcc<G * 4 + 3>(); return v;
}

__device__ __forceinline__ float rbf(float x) { return __bfloat162float(__float2bfloat16(x)); }

__global__ __launch_bounds__(256, 1) void wide_deep_kernel(
    const int* __restrict__ idx, const unsigned short* __restrict__ table, const unsigned short* __restrict__ xw,
    const unsigned short* __restrict__ ww, const unsigned short* __restrict__ bw, const unsigned short* __restrict__ W1,
    const unsigned short* __restrict__ b1, const unsigned short* __restrict__ W2, const unsigned short* __restrict__ b2,
    __hip_bfloat16* __restrict__ out, unsigned char* ws) {
  extern __shared__ __attribute__((aligned(16))) unsigned char lds[];
  const int tid = threadIdx.x, lane = tid & 63;
  const int w = __builtin_amdgcn_readfirstlane(tid >> 6);
  const int nt = blockIdx.x & 7, mt = blockIdx.x >> 3;
  const int n0 = nt * 64, m0 = mt * 128;
  const int fh = w >> 1, ks = w & 1;

  const unsigned epoch = __hip_atomic_load((gu32*)(ws + mt * 128), __ATOMIC_RELAXED, __HIP_MEMORY_SCOPE_AGENT);
  u32x2 b1v[8], w2v[8];
#pragma unroll
  for (int g = 0; g < 8; ++g) {
    const int n = n0 + (g >> 2) * 32 + (g & 3) * 8 + (lane >> 5) * 4;
    b1v[g] = *(const u32x2*)(b1 + n);
    w2v[g] = *(const u32x2*)(W2 + n);
  }
  const int frow = m0 + nt * 16 + (lane & 15);
  unsigned short xv[4], wv_[4];
#pragma unroll
  for (int i = 0; i < 4; ++i) {
    const int k = (lane >> 4) + 4 * i, kc = k < ND_ ? k : ND_ - 1;
    xv[i] = xw[frow * ND_ + kc]; wv_[i] = ww[kc];
  }
  unsigned short bwv = bw[(lane >> 6)], b2v = b2[(lane >> 6)];

  unsigned warm = 0;
  {
    const int t = tid < 208 ? tid : 207;
    const unsigned short* p = W1 + (size_t)(mt * 104 + (t >> 1)) * H_ + n0 + (t & 1) * 32;
    asm volatile("global_load_dword %0, %1, off" : "=v"(warm) : "v"(p) : "memory");
  }

  Ctx c;
  c.srdW = __builtin_amdgcn_make_buffer_rsrc((void*)W1, (short)0, F_ * D_ * H_ * 2, 0x00020000);
  c.srdT = __builtin_amdgcn_make_buffer_rsrc((void*)table, (short)0, V_ * D_ * 2, 0x00020000);
  const __amdgpu_buffer_rsrc_t srdI = __builtin_amdgcn_make_buffer_rsrc((void*)idx, (short)0, B_ * F_ * 4, 0x00020000);
  c.ring = RING0 + w * 8192;
  c.wbase = (unsigned)(fh * 13 * 128 + ks * 16) * 1024u;
  c.tgrp = (unsigned)w * 4096u;
  c.vW0 = (unsigned)(lane >> 3) * 1024u + (unsigned)(((lane & 7) ^ (((lane >> 4) & 1) << 2)) * 16) + (unsigned)n0 * 2u;
  c.vW1 = c.vW0 + 7168u;
  c.vT0 = (unsigned)(lane >> 2) * 256u + (unsigned)(((lane & 3) ^ ((lane >> 4) & 3)) * 16);
  c.vT1 = c.vT0 + 3072u; c.vT2 = c.vT0 + 6144u; c.vT3 = c.vT0 + 9216u;
  {
    const int qq = (lane & 15) >> 2, p = lane & 3, swz = (qq >> 1) & 1;
    const unsigned base = c.ring + (unsigned)((8 * (lane >> 5) + qq) * 128 + ((lane >> 4) & 1) * 32 + p * 8);
    c.tb0 = base + (unsigned)(swz * 64);
    c.tb1 = base + (unsigned)((swz ^ 1) * 64);
  }

  {
    const unsigned vI = (unsigned)lane * 16u;
#pragma unroll
    for (int i = 0; i < 4; ++i) {
      int p = w + 4 * i; p = p > 12 ? 12 : p;
      asm volatile("s_mov_b32 m0, %0\n\ts_nop 0\n\tbuffer_load_dwordx4 %1, %2, %3 offen lds"
                   ::"s"(65536u + (unsigned)p * 1024u), "v"(vI), "s"(srdI), "s"((unsigned)(m0 * F_ * 4 + p * 1024)) : "memory");
    }
  }
#pragma unroll
  for (int g = 0; g < 4; ++g) {
    dmaT<0>(c, c.tgrp + g * 16384u, c.tgrp * 4u + g * 65536u);
    dmaT<1>(c, c.tgrp + g * 16384u, c.tgrp * 4u + g * 65536u);
  }
#pragma unroll
  for (int j = 0; j < 4; ++j) dmaW(c, c.ring + j * 2048u, c.wbase + (unsigned)j * 131072u);

  asm volatile("" ::: ACLOB);
  Zero<0, 128>::run();

  waitV<24>();
  bar();
  {
    const int cch = ks * 2 + (lane >> 5);
#pragma unroll
    for (int i = 0; i < 4; ++i) {
      const int row = ((w + i) & 3) * 32 + (lane & 31);
      const int* ip = (const int*)(lds + 65536 + row * (F_ * 4) + fh * 52);
#pragma unroll
      for (int j = 0; j < 13; ++j) {
        const unsigned v = (unsigned)ip[j];
        c.addr[j][i] = v * 64u + (((unsigned)cch ^ ((v >> 2) & 3u)) << 4);
      }
    }
  }
  waitL<0>();
  waitV<6>();
  asm volatile("" ::"v"(warm));
  bar();
  readW<regW(2, 0), 0>(c.tb0);
  readW<regW(2, 1), 0>(c.tb1);
  readE<regE(2, 0)>(c.addr[0][0]); readE<regE(2, 1)>(c.addr[0][1]);
  readE<regE(2, 2)>(c.addr[0][2]); readE<regE(2, 3)>(c.addr[0][3]);

  unsigned slot = 0;
#pragma nounroll
  for (int q = 0; q < 3; ++q) phase<false>(c, q, slot);
  phase<true>(c, 3, slot);

  asm volatile("" : "+v"(xv[0]), "+v"(xv[1]), "+v"(xv[2]), "+v"(xv[3]), "+v"(wv_[0]), "+v"(wv_[1]), "+v"(wv_[2]),
                    "+v"(wv_[3]), "+v"(bwv), "+v"(b2v));
#pragma unroll
  for (int g = 0; g < 8; ++g) asm volatile("" : "+v"(b1v[g]), "+v"(w2v[g]));
  unsigned ep = epoch;
  asm volatile("" : "+v"(ep));
  asm volatile("s_nop 15\n\ts_waitcnt vmcnt(0) lgkmcnt(0)\n\ts_barrier" ::: "memory");
  sendBlock<1>((unsigned)((((w + 1) & 3) * 3 + 0) * 8192 + lane * 16));
  sendBlock<2>((unsigned)((((w + 2) & 3) * 3 + 1) * 8192 + lane * 16));
  sendBlock<3>((unsigned)((((w + 3) & 3) * 3 + 2) * 8192 + lane * 16));
  f32x4 h[8];
  h[0] = readAcc4<0>(); h[1] = readAcc4<1>(); h[2] = readAcc4<2>(); h[3] = readAcc4<3>();
  h[4] = readAcc4<4>(); h[5] = readAcc4<5>(); h[6] = readAcc4<6>(); h[7] = readAcc4<7>();
  asm volatile("s_waitcnt lgkmcnt(0)\n\ts_barrier" ::: "memory");
  float part = 0.f;
#pragma unroll
  for (int g = 0; g < 8; ++g) {
    const unsigned char* p = lds + w * 3 * 8192 + g * 1024 + lane * 16;
    const f32x4 s = h[g] + *(const f32x4*)p + *(const f32x4*)(p + 8192) + *(const f32x4*)(p + 16384);
    const unsigned bb[2] = {b1v[g].x, b1v[g].y}, wv[2] = {w2v[g].x, w2v[g].y};
#pragma unroll
    for (int e = 0; e < 4; ++e) {
      const float bias = __uint_as_float((e & 1) ? (bb[e >> 1] & 0xffff0000u) : (bb[e >> 1] << 16));
      const float w2 = __uint_as_float((e & 1) ? (wv[e >> 1] & 0xffff0000u) : (wv[e >> 1] << 16));
      float x = rbf(rbf(s[e]) + bias);
      x = x > 0.f ? x : x * 0.f;
      part = fmaf(x, w2, part);
    }
  }
  part += __shfl_xor(part, 32, 64);

  const unsigned tagA = ep + 1u, tagB = (ep + 1u) * 0x9E3779B1u ^ 0x5bd1e995u;
  gu64* gran = (gu64*)(ws + WS_GRAN);
  if (lane < 32) {
    gu64* g = gran + ((size_t)((mt * 8 + nt) * 128 + w * 32 + lane)) * 2;
    const unsigned pv = __float_as_uint(part);
    __hip_atomic_store(g, ((unsigned long long)tagA << 32) | pv, __ATOMIC_RELAXED, __HIP_MEMORY_SCOPE_AGENT);
    __hip_atomic_store(g + 1, ((unsigned long long)tagB << 32) | pv, __ATOMIC_RELAXED, __HIP_MEMORY_SCOPE_AGENT);
  }
  if (w == 0) {
    gu64* g0 = gran + ((size_t)((mt * 8 + (lane >> 4)) * 128 + nt * 16 + (lane & 15))) * 2;
    gu64* g1 = g0 + (size_t)4 * 128 * 2;
    float sum = 0.f, wide = 0.f;
#pragma unroll
    for (int i = 0; i < 4; ++i) {
      const float t = __uint_as_float((unsigned)xv[i] << 16) * __uint_as_float((unsigned)wv_[i] << 16);
      wide += ((lane >> 4) + 4 * i < ND_) ? t : 0.f;
    }
#pragma unroll 1
    for (unsigned spins = 0; spins < (1u << 22); ++spins) {
      const unsigned long long a0 = __hip_atomic_load(g0, __ATOMIC_RELAXED, __HIP_MEMORY_SCOPE_AGENT);
      const unsigned long long a1 = __hip_atomic_load(g0 + 1, __ATOMIC_RELAXED, __HIP_MEMORY_SCOPE_AGENT);
      const unsigned long long c0 = __hip_atomic_load(g1, __ATOMIC_RELAXED, __HIP_MEMORY_SCOPE_AGENT);
      const unsigned long long c1 = __hip_atomic_load(g1 + 1, __ATOMIC_RELAXED, __HIP_MEMORY_SCOPE_AGENT);
      const bool ok = (unsigned)(a0 >> 32) == tagA && (unsigned)(a1 >> 32) == tagB && (unsigned)(c0 >> 32) == tagA &&
                      (unsigned)(c1 >> 32) == tagB && (unsigned)a0 == (unsigned)a1 && (unsigned)c0 == (unsigned)c1;
      sum = __uint_as_float((unsigned)a0) + __uint_as_float((unsigned)c0);
      if (__all(ok)) break;
      __builtin_amdgcn_s_sleep(1);
    }
    sum += __shfl_xor(sum, 16, 64);
    sum += __shfl_xor(sum, 32, 64);
    wide += __shfl_xor(wide, 16, 64);
    wide += __shfl_xor(wide, 32, 64);
    if (lane < 16) {
      wide = rbf(rbf(wide) + __uint_as_float((unsigned)bwv << 16));
      const float deep = rbf(rbf(sum) + __uint_as_float((unsigned)b2v << 16));
      const float z = rbf(wide + deep);
      out[frow] = __float2bfloat16(1.f / (1.f + __expf(-z)));
    }
    if (nt == 0 && lane == 0) __hip_atomic_store((gu32*)(ws + mt * 128), tagA, __ATOMIC_RELAXED, __HIP_MEMORY_SCOPE_AGENT);
  }
}

extern "C" void kernel_launch(void* const* d_in, const int* in_sizes, int n_in, void* d_out, int out_size, void* d_ws,
                              size_t ws_size, hipStream_t stream) {
  (void)in_sizes; (void)n_in; (void)out_size;
  if (ws_size < (size_t)WS_GRAN + 32 * 8 * 128 * 16) return;
  (void)hipFuncSetAttribute((const void*)wide_deep_kernel, hipFuncAttributeMaxDynamicSharedMemorySize, LDS_BYTES);
  wide_deep_kernel<<<256, 256, LDS_BYTES, stream>>>(
      (const int*)d_in[0], (const unsigned short*)d_in[1], (const unsigned short*)d_in[2], (const unsigned short*)d_in[3],
      (const unsigned short*)d_in[4], (const unsigned short*)d_in[5], (const unsigned short*)d_in[6],
      (const unsigned short*)d_in[7], (const unsigned short*)d_in[8], (__hip_bfloat16*)d_out, (unsigned char*)d_ws);
}
```

```cpp
#include <hip/hip_runtime.h>
#include <hip/hip_bf16.h>
#include <stdint.h>

typedef unsigned u32x4 __attribute__((ext_vector_type(4)));
typedef unsigned u32x2 __attribute__((ext_vector_type(2)));
typedef float f32x4 __attribute__((ext_vector_type(4)));
typedef __attribute__((address_space(1))) unsigned long long gu64;
typedef __attribute__((address_space(1))) unsigned gu32;

constexpr int B_ = 4096, F_ = 26, D_ = 128, V_ = 1000, ND_ = 13, H_ = 512;
constexpr int LDS_BYTES = 163840;
constexpr int RING0 = 131072;
constexpr int WS_GRAN = 4096;

#define WREC (F_ * D_ * H_ * 2)
#define TREC (V_ * D_ * 2)
#define ACLOB "a0","a1","a2","a3","a4","a5","a6","a7","a8","a9","a10","a11","a12","a13","a14","a15", \
  "a16","a17","a18","a19","a20","a21","a22","a23","a24","a25","a26","a27","a28","a29","a30","a31", \
  "a32","a33","a34","a35","a36","a37","a38","a39","a40","a41","a42","a43","a44","a45","a46","a47", \
  "a48","a49","a50","a51","a52","a53","a54","a55","a56","a57","a58","a59","a60","a61","a62","a63", \
  "a64","a65","a66","a67","a68","a69","a70","a71","a72","a73","a74","a75","a76","a77","a78","a79", \
  "a80","a81","a82","a83","a84","a85","a86","a87","a88","a89","a90","a91","a92","a93","a94","a95", \
  "a96","a97","a98","a99","a100","a101","a102","a103","a104","a105","a106","a107","a108","a109","a110","a111", \
  "a112","a113","a114","a115","a116","a117","a118","a119","a120","a121","a122","a123","a124","a125","a126","a127", \
  "a128","a129","a130","a131","a132","a133","a134","a135","a136","a137","a138","a139","a140","a141","a142","a143", \
  "a144","a145","a146","a147","a148","a149","a150","a151","a152","a153","a154","a155","a156","a157","a158","a159", \
  "a160","a161","a162","a163","a164","a165","a166","a167","a168","a169","a170","a171","a172","a173","a174","a175", \
  "a176","a177","a178","a179","a180","a181","a182","a183","a184","a185","a186","a187","a188","a189","a190","a191", \
  "a192","a193","a194","a195","a196","a197","a198","a199"

struct Ctx {
  unsigned addr[13][4];
  unsigned tb0, tb1;
  unsigned vW0, vW1;
  unsigned vT[4];
  __amdgpu_buffer_rsrc_t srdW, srdT;
  unsigned ring;
  unsigned wbase;
  unsigned tgrp;
};

__host__ __device__ constexpr int setOf(int j) { return j == 0 ? 2 : (j & 1); }
__host__ __device__ constexpr int regE(int set, int i) { return 128 + set * 24 + i * 4; }
__host__ __device__ constexpr int regW(int set, int nb) { return 128 + set * 24 + 16 + nb * 4; }
__host__ __device__ constexpr int regC(int i, int nb) { return (i * 2 + nb) * 16; }

#define STAMP_AT(i) do {} while (0)
#define STAMP_RT(i) do {} while (0)
template <int N> __device__ __forceinline__ void waitV() { asm volatile("s_waitcnt vmcnt(%0)" ::"n"(N) : "memory"); }
template <int N> __device__ __forceinline__ void waitL() { asm volatile("s_waitcnt lgkmcnt(%0)" ::"n"(N) : "memory"); }
__device__ __forceinline__ void bar() { asm volatile("s_barrier" ::: "memory"); }

template <int R> __device__ __forceinline__ void readE(unsigned& a) {
  asm volatile("ds_read_b128 a[%c1:%c2], %0\n\tv_xor_b32 %0, 0x10000, %0" : "+v"(a) : "i"(R), "i"(R + 3) : "memory");
}
template <int R, int OFF> __device__ __forceinline__ void readW(unsigned a) {
  asm volatile("ds_read_b64_tr_b16 a[%c1:%c2], %0 offset:%c3\n\tds_read_b64_tr_b16 a[%c4:%c5], %0 offset:%c6"
               ::"v"(a), "i"(R), "i"(R + 1), "i"(OFF), "i"(R + 2), "i"(R + 3), "i"(OFF + 512) : "memory");
}
template <int C, int W, int E> __device__ __forceinline__ void mfma() {
  asm volatile("v_mfma_f32_32x32x16_bf16 a[%c0:%c1], a[%c2:%c3], a[%c4:%c5], a[%c0:%c1]"
               ::"i"(C), "i"(C + 15), "i"(W), "i"(W + 3), "i"(E), "i"(E + 3));
}
template <unsigned M, unsigned SO, int OFF>
__device__ __forceinline__ void dma1(unsigned m0a, unsigned voff, __amdgpu_buffer_rsrc_t srd, unsigned soa) {
  unsigned so;
  asm volatile("s_add_u32 m0, %1, %5\n\ts_add_u32 %0, %4, %6\n\tbuffer_load_dwordx4 %2, %3, %0 offen offset:%c7 lds"
               : "=&s"(so) : "s"(m0a), "v"(voff), "s"(srd), "s"(soa), "i"(M), "i"(SO), "i"(OFF) : "memory", "scc");
}

__host__ __device__ constexpr int nS(int j, bool last) { return (last || j < 0 || j > 10) ? 0 : (j == 10 ? 1 : 2 - (j & 1)); }
__host__ __device__ constexpr int firstS(int j) { int n = 0; for (int i = 0; i < j; ++i) n += nS(i, false); return n; }
__host__ __device__ constexpr int nB(int j, bool last) { return (!last || j <= 8) ? 2 : 0; }
__host__ __device__ constexpr int opsAll(int j, bool last) { return j < 0 ? 2 + nS(j + 13, false) : nB(j, last) + nS(j, last); }

template <int K, unsigned MADD, unsigned SADD> __device__ __forceinline__ void dmaT(const Ctx& c, unsigned m0a, unsigned soa) {
  constexpr int g = K >> 2, sub = K & 3;
  dma1<MADD + g * 16384u, SADD + g * 65536u, sub * 1024>(m0a, c.vT[sub], c.srdT, soa);
}

template <int J, bool LAST>
__device__ __forceinline__ void step(Ctx& c, unsigned wq, unsigned lb, unsigned tq, unsigned& slot) {
  constexpr int cur = setOf(J), JN = (J + 1) % 13, nxt = setOf(JN);
  constexpr bool hasNext = !(LAST && J == 12);
  constexpr int ns = nS(J, LAST), k0 = firstS(J);
  constexpr bool doW = nB(J, LAST) != 0;
  constexpr unsigned WO = (unsigned)((J + 4) % 13) * 131072u + ((J + 4) >= 13 ? 32768u : 0u);
  const unsigned slotN = (slot + 2048) & 8191;
  if constexpr (J == 12 && !LAST) { waitV<4>(); waitL<0>(); bar(); }
  else { if constexpr (hasNext) waitV<opsAll(J - 2, LAST) + opsAll(J - 1, LAST)>(); waitL<3>(); }
  const unsigned t0 = c.tb0 + slotN, t1 = c.tb1 + slotN, ms = c.ring + slot;
  mfma<regC(0, 0), regW(cur, 0), regE(cur, 0)>();
  if constexpr (hasNext) readW<regW(nxt, 0), 0>(t0);
  mfma<regC(0, 1), regW(cur, 1), regE(cur, 0)>();
  if constexpr (hasNext) readW<regW(nxt, 1), 0>(t1);
  if constexpr (hasNext) waitL<6>(); else waitL<2>();
  mfma<regC(1, 0), regW(cur, 0), regE(cur, 1)>();
  if constexpr (hasNext) readE<regE(nxt, 0)>(c.addr[JN][0]);
  if constexpr (ns >= 1) dmaT<k0, 0, 0>(c, lb, tq);
  mfma<regC(1, 1), regW(cur, 1), regE(cur, 1)>();
  if constexpr (doW) dma1<0, WO, 0>(ms, c.vW0, c.srdW, wq);
  if constexpr (hasNext) waitL<6>(); else waitL<1>();
  mfma<regC(2, 0), regW(cur, 0), regE(cur, 2)>();
  if constexpr (hasNext) readE<regE(nxt, 1)>(c.addr[JN][1]);
  if constexpr (ns >= 2) dmaT<k0 + 1, 0, 0>(c, lb, tq);
  mfma<regC(2, 1), regW(cur, 1), regE(cur, 2)>();
  if constexpr (doW) dma1<0, WO, 1024>(ms, c.vW1, c.srdW, wq);
  if constexpr (hasNext) waitL<6>(); else waitL<0>();
  mfma<regC(3, 0), regW(cur, 0), regE(cur, 3)>();
  if constexpr (hasNext) readE<regE(nxt, 2)>(c.addr[JN][2]);
  mfma<regC(3, 1), regW(cur, 1), regE(cur, 3)>();
  if constexpr (hasNext) readE<regE(nxt, 3)>(c.addr[JN][3]);
  slot = slotN;
}

template <bool LAST> __device__ __forceinline__ void phase(Ctx& c, int q, unsigned& slot) {
  const unsigned wq = c.wbase + (unsigned)q * 32768u, lb = (unsigned)(((q + 1) & 1) << 16) + c.tgrp,
                 tq = c.tgrp * 4u + (unsigned)(q + 1) * 64u;
  step<0, LAST>(c, wq, lb, tq, slot); step<1, LAST>(c, wq, lb, tq, slot); step<2, LAST>(c, wq, lb, tq, slot); step<3, LAST>(c, wq, lb, tq, slot);
  step<4, LAST>(c, wq, lb, tq, slot); step<5, LAST>(c, wq, lb, tq, slot); step<6, LAST>(c, wq, lb, tq, slot); step<7, LAST>(c, wq, lb, tq, slot);
  step<8, LAST>(c, wq, lb, tq, slot); step<9, LAST>(c, wq, lb, tq, slot); step<10, LAST>(c, wq, lb, tq, slot); step<11, LAST>(c, wq, lb, tq, slot);
  step<12, LAST>(c, wq, lb, tq, slot);
}

template <int R> __device__ __forceinline__ void zero1() { asm volatile("v_accvgpr_write_b32 a[%c0], 0" ::"i"(R)); }
template <int R, int N> struct Zero { static __device__ __forceinline__ void run() { zero1<R>(); Zero<R + 1, N - 1>::run(); } };
template <int R> struct Zero<R, 0> { static __device__ __forceinline__ void run() {} };

template <int R, int OFF> __device__ __forceinline__ void writeAcc(unsigned a) {
  asm volatile("ds_write_b128 %0, a[%c1:%c2] offset:%c3" ::"v"(a), "i"(R), "i"(R + 3), "i"(OFF) : "memory");
}
template <int I> __device__ __forceinline__ void sendBlock(unsigned a) {
  writeAcc<I * 32 + 0, 0>(a); writeAcc<I * 32 + 4, 1024>(a); writeAcc<I * 32 + 8, 2048>(a); writeAcc<I * 32 + 12, 3072>(a);
  writeAcc<I * 32 + 16, 4096>(a); writeAcc<I * 32 + 20, 5120>(a); writeAcc<I * 32 + 24, 6144>(a); writeAcc<I * 32 + 28, 7168>(a);
}
template <int R> __device__ __forceinline__ float readAcc() {
  float v; asm volatile("v_accvgpr_read_b32 %0, a[%c1]" : "=v"(v) : "i"(R)); return v;
}
template <int G> __device__ __forceinline__ f32x4 readAcc4() {
  f32x4 v; v.x = readAcc<G * 4>(); v.y = readAcc<G * 4 + 1>(); v.z = readAcc<G * 4 + 2>(); v.w = readAcc<G * 4 + 3>(); return v;
}

__device__ __forceinline__ float rbf(float x) { return __bfloat162float(__float2bfloat16(x)); }

__global__ __launch_bounds__(256, 1) void wide_deep_kernel(
    const int* __restrict__ idx, const unsigned short* __restrict__ table, const unsigned short* __restrict__ xw,
    const unsigned short* __restrict__ ww, const unsigned short* __restrict__ bw, const unsigned short* __restrict__ W1,
    const unsigned short* __restrict__ b1, const unsigned short* __restrict__ W2, const unsigned short* __restrict__ b2,
    __hip_bfloat16* __restrict__ out, unsigned char* ws) {
  extern __shared__ __attribute__((aligned(16))) unsigned char lds[];
  const int tid = threadIdx.x, lane = tid & 63;
  const int w = __builtin_amdgcn_readfirstlane(tid >> 6);
  const int nt = blockIdx.x & 7, mt = blockIdx.x >> 3;
  const int n0 = nt * 64, m0 = mt * 128;
  const int fh = w >> 1, ks = w & 1;

  STAMP_RT(12);
  STAMP_AT(0);

  Ctx c;
  const __amdgpu_buffer_rsrc_t srdI = __builtin_amdgcn_make_buffer_rsrc((void*)idx, (short)0, B_ * F_ * 4, 0x00020000);
  {
    const unsigned vI = (unsigned)lane * 16u, p3 = w == 0 ? 12u : (unsigned)w + 8u;
    dma1<65536u, 0, 0>((unsigned)w * 1024u, vI, srdI, (unsigned)(m0 * F_ * 4 + w * 1024));
    dma1<65536u + 4096u, 4096u, 0>((unsigned)w * 1024u, vI, srdI, (unsigned)(m0 * F_ * 4 + w * 1024));
    dma1<65536u, 0, 0>(p3 * 1024u, vI, srdI, (unsigned)(m0 * F_ * 4) + p3 * 1024u);
    dma1<65536u, 0, 0>((w == 0 ? 8u : 12u) * 1024u, vI, srdI, (unsigned)(m0 * F_ * 4) + (w == 0 ? 8u : 12u) * 1024u);
  }
  c.srdW = __builtin_amdgcn_make_buffer_rsrc((void*)W1, (short)0, WREC, 0x00020000);
  c.srdT = __builtin_amdgcn_make_buffer_rsrc((void*)table, (short)0, TREC, 0x00020000);
  c.ring = RING0 + w * 8192;
  c.wbase = (unsigned)(fh * 13 * 128 + ks * 16) * 1024u;
  c.tgrp = (unsigned)w * 4096u;
  c.vT[0] = (unsigned)(lane >> 2) * 256u + (unsigned)(((lane & 3) ^ ((lane >> 4) & 3)) * 16);
  c.vT[1] = c.vT[0] + 3072u; c.vT[2] = c.vT[0] + 6144u; c.vT[3] = c.vT[0] + 9216u;
  c.vW0 = (unsigned)(lane >> 3) * 1024u + (unsigned)(((lane & 7) ^ (((lane >> 4) & 1) << 2)) * 16) + (unsigned)n0 * 2u;
  c.vW1 = c.vW0 + 7168u;
  {
    const int qq = (lane & 15) >> 2, p = lane & 3, swz = (qq >> 1) & 1;
    const unsigned base = c.ring + (unsigned)((8 * (lane >> 5) + qq) * 128 + ((lane >> 4) & 1) * 32 + p * 8);
    c.tb0 = base + (unsigned)(swz * 64);
    c.tb1 = base + (unsigned)((swz ^ 1) * 64);
  }
  STAMP_AT(13);
  asm volatile("" ::: ACLOB);
  const unsigned tq0 = c.tgrp * 4u;
  dmaT<0, 0, 0>(c, c.tgrp, tq0); dmaT<1, 0, 0>(c, c.tgrp, tq0); Zero<0, 16>::run();
  dma1<0, 0, 0>(c.ring, c.vW0, c.srdW, c.wbase); dma1<0, 0, 1024>(c.ring, c.vW1, c.srdW, c.wbase); Zero<16, 16>::run();
  dmaT<2, 0, 0>(c, c.tgrp, tq0); dmaT<3, 0, 0>(c, c.tgrp, tq0); Zero<32, 16>::run();
  dmaT<4, 0, 0>(c, c.tgrp, tq0); dmaT<5, 0, 0>(c, c.tgrp, tq0); Zero<48, 16>::run();
  dmaT<6, 0, 0>(c, c.tgrp, tq0); dmaT<7, 0, 0>(c, c.tgrp, tq0); Zero<64, 16>::run();
  STAMP_AT(14);
  waitV<10>();
  bar();
  STAMP_AT(15);
  unsigned warm = 0;
  {
    const int t = tid < 208 ? tid : 207;
    const unsigned short* p = W1 + (size_t)(mt * 104 + (t >> 1)) * H_ + n0 + (t & 1) * 32;
    asm volatile("global_load_dword %0, %1, off" : "=v"(warm) : "v"(p) : "memory");
  }
  {
    const unsigned cs = (unsigned)(ks * 2 + (lane >> 5)) << 4;
#define ADDR_BLOCK(i)                                                                          \
    {                                                                                          \
      const int row = ((w + i) & 3) * 32 + (lane & 31);                                        \
      const int* ip = (const int*)(lds + 65536 + row * (F_ * 4) + fh * 52);                    \
      unsigned v[13];                                                                          \
      _Pragma("unroll") for (int j = 0; j < 13; ++j) v[j] = (unsigned)ip[j];                   \
      dmaT<8 + 2 * i, 0, 0>(c, c.tgrp, tq0); dmaT<9 + 2 * i, 0, 0>(c, c.tgrp, tq0);            \
      Zero<80 + 12 * i, 12>::run();                                                            \
      _Pragma("unroll") for (int j = 0; j < 13; ++j)                                           \
        c.addr[j][i] = (v[j] << 6) | (((v[j] << 2) & 0x30u) ^ cs);                             \
    }
    ADDR_BLOCK(0) ADDR_BLOCK(1) ADDR_BLOCK(2) ADDR_BLOCK(3)
#undef ADDR_BLOCK
  }
  STAMP_AT(16);
  dma1<2048, 131072, 0>(c.ring, c.vW0, c.srdW, c.wbase); dma1<2048, 131072, 1024>(c.ring, c.vW1, c.srdW, c.wbase);
  unsigned epoch;
  u32x2 b1v[8], w2v[8];
  unsigned xv[4], wv_[2], bwv, b2v;
  {
    const unsigned short* pb = b1 + n0 + (lane >> 5) * 4;
    const unsigned short* pw = W2 + n0 + (lane >> 5) * 4;
#define LD2(dst, ptr, off) asm volatile("global_load_dwordx2 %0, %1, off offset:" #off : "=v"(dst) : "v"(ptr) : "memory")
#define LDH(dst, ptr, off) asm volatile("global_load_ushort %0, %1, off offset:" #off : "=v"(dst) : "v"(ptr) : "memory")
    LD2(b1v[0], pb, 0); LD2(b1v[1], pb, 16); LD2(b1v[2], pb, 32); LD2(b1v[3], pb, 48);
    LD2(b1v[4], pb, 64); LD2(b1v[5], pb, 80); LD2(b1v[6], pb, 96); LD2(b1v[7], pb, 112);
    LD2(w2v[0], pw, 0); LD2(w2v[1], pw, 16); LD2(w2v[2], pw, 32); LD2(w2v[3], pw, 48);
    LD2(w2v[4], pw, 64); LD2(w2v[5], pw, 80); LD2(w2v[6], pw, 96); LD2(w2v[7], pw, 112);
    const int g = lane >> 3, g2 = g + 8 < ND_ ? g + 8 : ND_ - 1;
    const unsigned short* px = xw + (size_t)(m0 + nt * 16 + (lane & 7) * 2) * ND_;
    const unsigned short *px0 = px + g, *px1 = px + g2, *pw0 = ww + g, *pw1 = ww + g2;
    LDH(xv[0], px0, 0); LDH(xv[1], px0, 26); LDH(xv[2], px1, 0); LDH(xv[3], px1, 26);
    LDH(wv_[0], pw0, 0); LDH(wv_[1], pw1, 0); LDH(bwv, bw, 0); LDH(b2v, b2, 0);
    const unsigned char* pe = ws + mt * 128;
    asm volatile("global_load_dword %0, %1, off sc1" : "=v"(epoch) : "v"(pe) : "memory");
#undef LD2
#undef LDH
  }
  dma1<4096, 2 * 131072, 0>(c.ring, c.vW0, c.srdW, c.wbase); dma1<4096, 2 * 131072, 1024>(c.ring, c.vW1, c.srdW, c.wbase);
  dma1<6144, 3 * 131072, 0>(c.ring, c.vW0, c.srdW, c.wbase); dma1<6144, 3 * 131072, 1024>(c.ring, c.vW1, c.srdW, c.wbase);
  STAMP_AT(1);
  waitL<0>();
  waitV<2 + 25 + 4>();
  asm volatile("" ::"v"(warm));
  bar();
  STAMP_AT(3);
  readW<regW(2, 0), 0>(c.tb0);
  readW<regW(2, 1), 0>(c.tb1);
  readE<regE(2, 0)>(c.addr[0][0]); readE<regE(2, 1)>(c.addr[0][1]);
  readE<regE(2, 2)>(c.addr[0][2]); readE<regE(2, 3)>(c.addr[0][3]);

  unsigned slot = 0;
#pragma nounroll
  for (int q = 0; q < 3; ++q) {
    phase<false>(c, q, slot);
  }
  phase<true>(c, 3, slot);
  STAMP_AT(7);

  asm volatile("s_nop 15\n\ts_waitcnt vmcnt(0) lgkmcnt(0)\n\ts_barrier" ::: "memory");
  sendBlock<1>((unsigned)((((w + 1) & 3) * 3 + 0) * 8192 + lane * 16));
  sendBlock<2>((unsigned)((((w + 2) & 3) * 3 + 1) * 8192 + lane * 16));
  sendBlock<3>((unsigned)((((w + 3) & 3) * 3 + 2) * 8192 + lane * 16));
#pragma unroll
  for (int g = 0; g < 8; ++g) asm volatile("" : "+v"(b1v[g]), "+v"(w2v[g]));
  asm volatile("" : "+v"(xv[0]), "+v"(xv[1]), "+v"(xv[2]), "+v"(xv[3]), "+v"(wv_[0]), "+v"(wv_[1]), "+v"(bwv), "+v"(b2v), "+v"(epoch));
  f32x4 h[8], w2f[8];
  h[0] = readAcc4<0>(); h[1] = readAcc4<1>(); h[2] = readAcc4<2>(); h[3] = readAcc4<3>();
  h[4] = readAcc4<4>(); h[5] = readAcc4<5>(); h[6] = readAcc4<6>(); h[7] = readAcc4<7>();
#pragma unroll
  for (int g = 0; g < 8; ++g) {
    h[g].x += __uint_as_float(b1v[g].x << 16); h[g].y += __uint_as_float(b1v[g].x & 0xffff0000u);
    h[g].z += __uint_as_float(b1v[g].y << 16); h[g].w += __uint_as_float(b1v[g].y & 0xffff0000u);
    w2f[g].x = __uint_as_float(w2v[g].x << 16); w2f[g].y = __uint_as_float(w2v[g].x & 0xffff0000u);
    w2f[g].z = __uint_as_float(w2v[g].y << 16); w2f[g].w = __uint_as_float(w2v[g].y & 0xffff0000u);
    asm volatile("" : "+v"(h[g]), "+v"(w2f[g]));
  }
  float wide = __uint_as_float(xv[0] << 16) * __uint_as_float(wv_[0] << 16), wide1 = __uint_as_float(xv[1] << 16) * __uint_as_float(wv_[0] << 16);
  if ((lane >> 3) + 8 < ND_) {
    wide += __uint_as_float(xv[2] << 16) * __uint_as_float(wv_[1] << 16);
    wide1 += __uint_as_float(xv[3] << 16) * __uint_as_float(wv_[1] << 16);
  }
  asm volatile("s_waitcnt lgkmcnt(0)\n\ts_barrier" : "+v"(wide), "+v"(wide1) :: "memory");
  STAMP_AT(8);
  f32x4 pacc = {0.f, 0.f, 0.f, 0.f};
#pragma unroll
  for (int g = 0; g < 8; ++g) {
    const unsigned char* p = lds + w * 3 * 8192 + g * 1024 + lane * 16;
    const f32x4 s = (h[g] + *(const f32x4*)p) + (*(const f32x4*)(p + 8192) + *(const f32x4*)(p + 16384));
    pacc.x = fmaf(fmaxf(s.x, 0.f), w2f[g].x, pacc.x); pacc.y = fmaf(fmaxf(s.y, 0.f), w2f[g].y, pacc.y);
    pacc.z = fmaf(fmaxf(s.z, 0.f), w2f[g].z, pacc.z); pacc.w = fmaf(fmaxf(s.w, 0.f), w2f[g].w, pacc.w);
  }
  float part = (pacc.x + pacc.y) + (pacc.z + pacc.w);
  part += __shfl_xor(part, 32, 64);

  const unsigned tag = epoch + 1u;
  gu64* gran = (gu64*)(ws + WS_GRAN);
  if (lane < 32)
    __hip_atomic_store(gran + (size_t)((mt * 8 + nt) * 128 + w * 32 + lane), ((unsigned long long)tag << 32) | __float_as_uint(part),
                       __ATOMIC_RELAXED, __HIP_MEMORY_SCOPE_AGENT);
  STAMP_AT(9);
  if (w == 0) {
    const unsigned char* gp = ws + WS_GRAN + (size_t)((mt * 8 + (lane >> 3)) * 128 + nt * 16 + (lane & 7) * 2) * 8;
    u32x4 gv;
#pragma unroll 1
    for (unsigned spins = 0; spins < (1u << 22); ++spins) {
      asm volatile("global_load_dwordx4 %0, %1, off sc1\n\ts_waitcnt vmcnt(0)" : "=v"(gv) : "v"(gp) : "memory");
      if (__all(gv.y == tag && gv.w == tag)) break;
    }
    float d0 = __uint_as_float(gv.x), d1 = __uint_as_float(gv.z);
#pragma unroll
    for (int m = 8; m < 64; m <<= 1) {
      d0 += __shfl_xor(d0, m, 64); d1 += __shfl_xor(d1, m, 64);
      wide += __shfl_xor(wide, m, 64); wide1 += __shfl_xor(wide1, m, 64);
    }
    if (lane < 8) {
      const float bwf = __uint_as_float(bwv << 16), b2f = __uint_as_float(b2v << 16);
      const float z0 = rbf(rbf(rbf(wide) + bwf) + rbf(rbf(d0) + b2f)), z1 = rbf(rbf(rbf(wide1) + bwf) + rbf(rbf(d1) + b2f));
      __hip_bfloat162 o;
      o.x = __float2bfloat16(1.f / (1.f + __expf(-z0)));
      o.y = __float2bfloat16(1.f / (1.f + __expf(-z1)));
      *(__hip_bfloat162*)(out + m0 + nt * 16 + lane * 2) = o;
    }
    if (nt == 0 && lane == 0) __hip_atomic_store((gu32*)(ws + mt * 128), tag, __ATOMIC_RELAXED, __HIP_MEMORY_SCOPE_AGENT);
  }
}

extern "C" void kernel_launch(void* const* d_in, const int* in_sizes, int n_in, void* d_out, int out_size, void* d_ws,
                              size_t ws_size, hipStream_t stream) {
  (void)in_sizes; (void)n_in; (void)out_size;
  if (ws_size < (size_t)WS_GRAN + 32 * 8 * 128 * 8) return;
  (void)hipFuncSetAttribute((const void*)wide_deep_kernel, hipFuncAttributeMaxDynamicSharedMemorySize, LDS_BYTES);
  wide_deep_kernel<<<256, 256, LDS_BYTES, stream>>>(
      (const int*)d_in[0], (const unsigned short*)d_in[1], (const unsigned short*)d_in[2], (const unsigned short*)d_in[3],
      (const unsigned short*)d_in[4], (const unsigned short*)d_in[5], (const unsigned short*)d_in[6],
      (const unsigned short*)d_in[7], (const unsigned short*)d_in[8], (__hip_bfloat16*)d_out, (unsigned char*)d_ws);
}
```

```cpp
#include <hip/hip_runtime.h>
#include <hip/hip_bf16.h>
#include <stdint.h>

typedef unsigned u32x4 __attribute__((ext_vector_type(4)));
typedef unsigned u32x2 __attribute__((ext_vector_type(2)));
typedef float f32x4 __attribute__((ext_vector_type(4)));
typedef __attribute__((address_space(1))) unsigned long long gu64;
typedef __attribute__((address_space(1))) unsigned gu32;

constexpr int B_ = 4096, F_ = 26, D_ = 128, V_ = 1000, ND_ = 13, H_ = 512;
constexpr int LDS_BYTES = 163840;
constexpr int RING0 = 131072;
constexpr int SMALL0 = 64512;
constexpr int RED0 = 65536;
constexpr int WS_GRAN = 4096;

#define WREC (F_ * D_ * H_ * 2)
#define TREC (V_ * D_ * 2)
#define ACLOB "a0","a1","a2","a3","a4","a5","a6","a7","a8","a9","a10","a11","a12","a13","a14","a15", \
  "a16","a17","a18","a19","a20","a21","a22","a23","a24","a25","a26","a27","a28","a29","a30","a31", \
  "a32","a33","a34","a35","a36","a37","a38","a39","a40","a41","a42","a43","a44","a45","a46","a47", \
  "a48","a49","a50","a51","a52","a53","a54","a55","a56","a57","a58","a59","a60","a61","a62","a63", \
  "a64","a65","a66","a67","a68","a69","a70","a71","a72","a73","a74","a75","a76","a77","a78","a79", \
  "a80","a81","a82","a83","a84","a85","a86","a87","a88","a89","a90","a91","a92","a93","a94","a95", \
  "a96","a97","a98","a99","a100","a101","a102","a103","a104","a105","a106","a107","a108","a109","a110","a111", \
  "a112","a113","a114","a115","a116","a117","a118","a119","a120","a121","a122","a123","a124","a125","a126","a127", \
  "a128","a129","a130","a131","a132","a133","a134","a135","a136","a137","a138","a139","a140","a141","a142","a143", \
  "a144","a145","a146","a147","a148","a149","a150","a151","a152","a153","a154","a155","a156","a157","a158","a159", \
  "a160","a161","a162","a163","a164","a165","a166","a167","a168","a169","a170","a171","a172","a173","a174","a175", \
  "a176","a177","a178","a179","a180","a181","a182","a183","a184","a185","a186","a187","a188","a189","a190","a191", \
  "a192","a193","a194","a195","a196","a197","a198","a199"

struct Ctx {
  unsigned addr[13][4];
  unsigned tb0, tb1;
  unsigned vW0, vW1;
  unsigned vT[4];
  __amdgpu_buffer_rsrc_t srdW, srdT;
  unsigned ring;
  unsigned wbase;
  unsigned tgrp;
};

__host__ __device__ constexpr int setOf(int j) { return j == 0 ? 2 : (j & 1); }
__host__ __device__ constexpr int regE(int set, int i) { return 128 + set * 24 + i * 4; }
__host__ __device__ constexpr int regW(int set, int nb) { return 128 + set * 24 + 16 + nb * 4; }
__host__ __device__ constexpr int regC(int i, int nb) { return (i * 2 + nb) * 16; }

#define STAMP_AT(i) do {} while (0)
#define STAMP_RT(i) do {} while (0)
template <int N> __device__ __forceinline__ void waitV() { asm volatile("s_waitcnt vmcnt(%0)" ::"n"(N) : "memory"); }
template <int N> __device__ __forceinline__ void waitL() { asm volatile("s_waitcnt lgkmcnt(%0)" ::"n"(N) : "memory"); }
__device__ __forceinline__ void bar() { asm volatile("s_barrier" ::: "memory"); }

template <int R> __device__ __forceinline__ void readE(unsigned& a) {
  asm volatile("ds_read_b128 a[%c1:%c2], %0\n\tv_xor_b32 %0, 0x10000, %0" : "+v"(a) : "i"(R), "i"(R + 3) : "memory");
}
template <int R, int OFF> __device__ __forceinline__ void readW(unsigned a) {
  asm volatile("ds_read_b64_tr_b16 a[%c1:%c2], %0 offset:%c3\n\tds_read_b64_tr_b16 a[%c4:%c5], %0 offset:%c6"
               ::"v"(a), "i"(R), "i"(R + 1), "i"(OFF), "i"(R + 2), "i"(R + 3), "i"(OFF + 512) : "memory");
}
template <int C, int W, int E> __device__ __forceinline__ void mfma() {
  asm volatile("v_mfma_f32_32x32x16_bf16 a[%c0:%c1], a[%c2:%c3], a[%c4:%c5], a[%c0:%c1]"
               ::"i"(C), "i"(C + 15), "i"(W), "i"(W + 3), "i"(E), "i"(E + 3));
}
template <unsigned M, unsigned SO, int OFF>
__device__ __forceinline__ void dma1(unsigned m0a, unsigned voff, __amdgpu_buffer_rsrc_t srd, unsigned soa) {
  unsigned so;
  asm volatile("s_add_u32 m0, %1, %5\n\ts_add_u32 %0, %4, %6\n\tbuffer_load_dwordx4 %2, %3, %0 offen offset:%c7 lds"
               : "=&s"(so) : "s"(m0a), "v"(voff), "s"(srd), "s"(soa), "i"(M), "i"(SO), "i"(OFF) : "memory", "scc");
}

__host__ __device__ constexpr int nS(int j, bool last) { return (last || j < 0 || j > 10) ? 0 : (j == 10 ? 1 : 2 - (j & 1)); }
__host__ __device__ constexpr int firstS(int j) { int n = 0; for (int i = 0; i < j; ++i) n += nS(i, false); return n; }
__host__ __device__ constexpr int nB(int j, bool last) { return (!last || j <= 8) ? 2 : 0; }
__host__ __device__ constexpr int opsAll(int j, bool last) { return j < 0 ? 2 + nS(j + 13, false) : nB(j, last) + nS(j, last); }

template <int K> __device__ __forceinline__ void dmaT(const Ctx& c, unsigned m0a, unsigned soa) {
  constexpr int g = K >> 2, sub = K & 3;
  if constexpr (K == 15) m0a = c.tgrp == 3 * 4096u ? 130048u - 52224u : m0a;
  dma1<g * 16384u, g * 65536u, sub * 1024>(m0a, c.vT[sub], c.srdT, soa);
}

template <int J, bool LAST>
__device__ __forceinline__ void step(Ctx& c, unsigned wq, unsigned lb, unsigned tq, unsigned& slot) {
  constexpr int cur = setOf(J), JN = (J + 1) % 13, nxt = setOf(JN);
  constexpr bool hasNext = !(LAST && J == 12);
  constexpr int ns = nS(J, LAST), k0 = firstS(J);
  constexpr bool doW = nB(J, LAST) != 0;
  constexpr unsigned WO = (unsigned)((J + 4) % 13) * 131072u + ((J + 4) >= 13 ? 32768u : 0u);
  const unsigned slotN = (slot + 2048) & 8191;
  if constexpr (J == 12 && !LAST) { waitV<4>(); waitL<0>(); bar(); }
  else { if constexpr (hasNext) waitV<opsAll(J - 2, LAST) + opsAll(J - 1, LAST)>(); waitL<3>(); }
  const unsigned t0 = c.tb0 + slotN, t1 = c.tb1 + slotN, ms = c.ring + slot;
  mfma<regC(0, 0), regW(cur, 0), regE(cur, 0)>();
  if constexpr (hasNext) readW<regW(nxt, 0), 0>(t0);
  mfma<regC(0, 1), regW(cur, 1), regE(cur, 0)>();
  if constexpr (hasNext) readW<regW(nxt, 1), 0>(t1);
  if constexpr (hasNext) waitL<6>(); else waitL<2>();
  mfma<regC(1, 0), regW(cur, 0), regE(cur, 1)>();
  if constexpr (hasNext) readE<regE(nxt, 0)>(c.addr[JN][0]);
  if constexpr (ns >= 1) dmaT<k0>(c, lb, tq);
  mfma<regC(1, 1), regW(cur, 1), regE(cur, 1)>();
  if constexpr (doW) dma1<0, WO, 0>(ms, c.vW0, c.srdW, wq);
  if constexpr (hasNext) waitL<6>(); else waitL<1>();
  mfma<regC(2, 0), regW(cur, 0), regE(cur, 2)>();
  if constexpr (hasNext) readE<regE(nxt, 1)>(c.addr[JN][1]);
  if constexpr (ns >= 2) dmaT<k0 + 1>(c, lb, tq);
  mfma<regC(2, 1), regW(cur, 1), regE(cur, 2)>();
  if constexpr (doW) dma1<0, WO, 1024>(ms, c.vW1, c.srdW, wq);
  if constexpr (hasNext) waitL<6>(); else waitL<0>();
  mfma<regC(3, 0), regW(cur, 0), regE(cur, 3)>();
  if constexpr (hasNext) readE<regE(nxt, 2)>(c.addr[JN][2]);
  mfma<regC(3, 1), regW(cur, 1), regE(cur, 3)>();
  if constexpr (hasNext) readE<regE(nxt, 3)>(c.addr[JN][3]);
  slot = slotN;
}

template <bool LAST> __device__ __forceinline__ void phase(Ctx& c, int q, unsigned& slot) {
  const unsigned wq = c.wbase + (unsigned)q * 32768u, lb = (unsigned)(((q + 1) & 1) << 16) + c.tgrp,
                 tq = c.tgrp * 4u + (unsigned)(q + 1) * 64u;
  step<0, LAST>(c, wq, lb, tq, slot); step<1, LAST>(c, wq, lb, tq, slot); step<2, LAST>(c, wq, lb, tq, slot); step<3, LAST>(c, wq, lb, tq, slot);
  step<4, LAST>(c, wq, lb, tq, slot); step<5, LAST>(c, wq, lb, tq, slot); step<6, LAST>(c, wq, lb, tq, slot); step<7, LAST>(c, wq, lb, tq, slot);
  step<8, LAST>(c, wq, lb, tq, slot); step<9, LAST>(c, wq, lb, tq, slot); step<10, LAST>(c, wq, lb, tq, slot); step<11, LAST>(c, wq, lb, tq, slot);
  step<12, LAST>(c, wq, lb, tq, slot);
}

template <int R> __device__ __forceinline__ void zero1() { asm volatile("v_accvgpr_write_b32 a[%c0], 0" ::"i"(R)); }
template <int R, int N> struct Zero { static __device__ __forceinline__ void run() { zero1<R>(); Zero<R + 1, N - 1>::run(); } };
template <int R> struct Zero<R, 0> { static __device__ __forceinline__ void run() {} };

template <int R, int OFF> __device__ __forceinline__ void writeAcc(unsigned a) {
  asm volatile("ds_write_b128 %0, a[%c1:%c2] offset:%c3" ::"v"(a), "i"(R), "i"(R + 3), "i"(OFF) : "memory");
}
template <int R> __device__ __forceinline__ float readAcc() {
  float v; asm volatile("v_accvgpr_read_b32 %0, a[%c1]" : "=v"(v) : "i"(R)); return v;
}
template <int G> __device__ __forceinline__ f32x4 readAcc4() {
  f32x4 v; v.x = readAcc<G * 4>(); v.y = readAcc<G * 4 + 1>(); v.z = readAcc<G * 4 + 2>(); v.w = readAcc<G * 4 + 3>(); return v;
}

__device__ __forceinline__ float sumX8(float x) {
  return x + __int_as_float(__builtin_amdgcn_update_dpp(0, __float_as_int(x), 0x128, 0xf, 0xf, true));
}
__device__ __forceinline__ float sumX16(float x) {
  const auto r = __builtin_amdgcn_permlane16_swap(__float_as_uint(x), __float_as_uint(x), false, false);
  return __uint_as_float(r[0]) + __uint_as_float(r[1]);
}
__device__ __forceinline__ float sumX32(float x) {
  const auto r = __builtin_amdgcn_permlane32_swap(__float_as_uint(x), __float_as_uint(x), false, false);
  return __uint_as_float(r[0]) + __uint_as_float(r[1]);
}
__device__ __forceinline__ float rbf(float x) { return __bfloat162float(__float2bfloat16(x)); }

__global__ __launch_bounds__(256, 1) void wide_deep_kernel(
    const int* __restrict__ idx, const unsigned short* __restrict__ table, const unsigned short* __restrict__ xw,
    const unsigned short* __restrict__ ww, const unsigned short* __restrict__ bw, const unsigned short* __restrict__ W1,
    const unsigned short* __restrict__ b1, const unsigned short* __restrict__ W2, const unsigned short* __restrict__ b2,
    __hip_bfloat16* __restrict__ out, unsigned char* ws) {
  extern __shared__ __attribute__((aligned(16))) unsigned char lds[];
  const int tid = threadIdx.x, lane = tid & 63;
  const int w = __builtin_amdgcn_readfirstlane(tid >> 6);
  const int nt = blockIdx.x & 7, mt = blockIdx.x >> 3;
  const int n0 = nt * 64, m0 = mt * 128;
  const int fh = w >> 1, ks = w & 1;

  STAMP_RT(12);
  STAMP_AT(0);

  asm volatile("" ::"s"(idx), "s"(table), "s"(xw), "s"(ww), "s"(bw), "s"(W1), "s"(b1), "s"(W2), "s"(b2), "s"(out), "s"(ws));
  unsigned epoch, wv_[2] = {0, 0}, bwv = 0, b2v = 0;
#define LDH(dst, ptr) asm volatile("global_load_ushort %0, %1, off" : "=v"(dst) : "v"(ptr) : "memory")
  asm volatile("global_load_dword %0, %1, off sc1" : "=v"(epoch) : "v"(ws + mt * 128) : "memory");
  if (w == 0) {
    const int g = lane >> 3, g2 = g + 8 < ND_ ? g + 8 : ND_ - 1;
    LDH(wv_[0], ww + g); LDH(wv_[1], ww + g2); LDH(bwv, bw); LDH(b2v, b2);
  }
#undef LDH
  Ctx c;
  const __amdgpu_buffer_rsrc_t srdI = __builtin_amdgcn_make_buffer_rsrc((void*)idx, (short)0, B_ * F_ * 4, 0x00020000);
  {
    const unsigned vI = (unsigned)lane * 16u, p3 = w == 0 ? 12u : (unsigned)w + 8u;
    dma1<65536u, 0, 0>((unsigned)w * 1024u, vI, srdI, (unsigned)(m0 * F_ * 4 + w * 1024));
    dma1<65536u + 4096u, 4096u, 0>((unsigned)w * 1024u, vI, srdI, (unsigned)(m0 * F_ * 4 + w * 1024));
    dma1<65536u, 0, 0>(p3 * 1024u, vI, srdI, (unsigned)(m0 * F_ * 4) + p3 * 1024u);
    dma1<65536u, 0, 0>((w == 0 ? 8u : 12u) * 1024u, vI, srdI, (unsigned)(m0 * F_ * 4) + (w == 0 ? 8u : 12u) * 1024u);
  }
  unsigned warm = 0, warm2 = 0;
  {
    const int ln = mt * 64 + (tid >> 2);
    const unsigned short* p2 = table + (size_t)(ln < 2000 ? ln : 1999) * 64;
    const int t = tid < 208 ? tid : 207;
    const unsigned short* p = W1 + (size_t)(mt * 104 + (t >> 1)) * H_ + n0 + (t & 1) * 32;
    asm volatile("global_load_dword %0, %2, off\n\tglobal_load_dword %1, %3, off"
                 : "=&v"(warm2), "=&v"(warm) : "v"(p2), "v"(p) : "memory");
  }
  c.srdW = __builtin_amdgcn_make_buffer_rsrc((void*)W1, (short)0, WREC, 0x00020000);
  c.srdT = __builtin_amdgcn_make_buffer_rsrc((void*)table, (short)0, TREC, 0x00020000);
  c.ring = RING0 + w * 8192;
  c.wbase = (unsigned)(fh * 13 * 128 + ks * 16) * 1024u;
  c.tgrp = (unsigned)w * 4096u;
  c.vT[0] = (unsigned)(lane >> 2) * 256u + (unsigned)(((lane & 3) ^ ((lane >> 4) & 3)) * 16);
  c.vT[1] = c.vT[0] + 3072u; c.vT[2] = c.vT[0] + 6144u; c.vT[3] = c.vT[0] + 9216u;
  c.vW0 = (unsigned)(lane >> 3) * 1024u + (unsigned)(((lane & 7) ^ (((lane >> 4) & 1) << 2)) * 16) + (unsigned)n0 * 2u;
  c.vW1 = c.vW0 + 7168u;
  {
    const int qq = (lane & 15) >> 2, p = lane & 3, swz = (qq >> 1) & 1;
    const unsigned base = c.ring + (unsigned)((8 * (lane >> 5) + qq) * 128 + ((lane >> 4) & 1) * 32 + p * 8);
    c.tb0 = base + (unsigned)(swz * 64);
    c.tb1 = base + (unsigned)((swz ^ 1) * 64);
  }
  STAMP_AT(13);
  asm volatile("" ::: ACLOB);
  const unsigned tq0 = c.tgrp * 4u;
  dmaT<0>(c, c.tgrp, tq0); dmaT<1>(c, c.tgrp, tq0); Zero<0, 16>::run();
  dma1<0, 0, 0>(c.ring, c.vW0, c.srdW, c.wbase); dma1<0, 0, 1024>(c.ring, c.vW1, c.srdW, c.wbase); Zero<16, 16>::run();
  dmaT<2>(c, c.tgrp, tq0); dmaT<3>(c, c.tgrp, tq0); Zero<32, 16>::run();
  dmaT<4>(c, c.tgrp, tq0); dmaT<5>(c, c.tgrp, tq0); Zero<48, 16>::run();
  dmaT<6>(c, c.tgrp, tq0); dmaT<7>(c, c.tgrp, tq0); Zero<64, 16>::run();
  STAMP_AT(14);
  waitV<12>();
  bar();
  STAMP_AT(15);
  unsigned warm3 = 0;
  asm volatile("global_load_dword %0, %1, off" : "=v"(warm3) : "v"(out + m0 + nt * 16) : "memory");
  {
    const unsigned cs = (unsigned)(ks * 2 + (lane >> 5)) << 4;
#define ADDR_BLOCK(i)                                                                          \
    {                                                                                          \
      const int row = ((w + i) & 3) * 32 + (lane & 31);                                        \
      const int* ip = (const int*)(lds + 65536 + row * (F_ * 4) + fh * 52);                    \
      unsigned v[13];                                                                          \
      _Pragma("unroll") for (int j = 0; j < 13; ++j) v[j] = (unsigned)ip[j];                   \
      dmaT<8 + 2 * i>(c, c.tgrp, tq0); dmaT<9 + 2 * i>(c, c.tgrp, tq0);                  \
      Zero<80 + 12 * i, 12>::run();                                                            \
      _Pragma("unroll") for (int j = 0; j < 13; ++j)                                           \
        c.addr[j][i] = (v[j] << 6) | (((v[j] << 2) & 0x30u) ^ cs);                             \
    }
    ADDR_BLOCK(0) ADDR_BLOCK(1) ADDR_BLOCK(2) ADDR_BLOCK(3)
#undef ADDR_BLOCK
  }
  STAMP_AT(16);
  dma1<2048, 131072, 0>(c.ring, c.vW0, c.srdW, c.wbase); dma1<2048, 131072, 1024>(c.ring, c.vW1, c.srdW, c.wbase);
  {
    const unsigned char* src = lane >= 16 && lane < 42 ? (const unsigned char*)(xw + (size_t)(m0 + nt * 16) * ND_) + (lane - 16) * 16
                               : (const unsigned char*)((lane >> 3) == 1 ? W2 : b1) + n0 * 2 + (lane & 7) * 16;
    asm volatile("s_mov_b32 m0, %0\n\ts_nop 0\n\tglobal_load_lds_dwordx4 %1, off" ::"s"(SMALL0), "v"(src) : "memory");
  }
  dma1<4096, 2 * 131072, 0>(c.ring, c.vW0, c.srdW, c.wbase); dma1<4096, 2 * 131072, 1024>(c.ring, c.vW1, c.srdW, c.wbase);
  dma1<6144, 3 * 131072, 0>(c.ring, c.vW0, c.srdW, c.wbase); dma1<6144, 3 * 131072, 1024>(c.ring, c.vW1, c.srdW, c.wbase);
  STAMP_AT(1);
  waitL<0>();
  waitV<2 + 1 + 4>();
  asm volatile("" ::"v"(warm), "v"(warm2), "v"(warm3));
  bar();
  STAMP_AT(3);
  readW<regW(2, 0), 0>(c.tb0);
  readW<regW(2, 1), 0>(c.tb1);
  readE<regE(2, 0)>(c.addr[0][0]); readE<regE(2, 1)>(c.addr[0][1]);
  readE<regE(2, 2)>(c.addr[0][2]); readE<regE(2, 3)>(c.addr[0][3]);

  unsigned slot = 0;
#pragma nounroll
  for (int q = 0; q < 3; ++q) {
    phase<false>(c, q, slot);
  }
  u32x2 b1v[8], w2v[8];
  unsigned xv[4];
  {
    const unsigned char* sp = lds + SMALL0 + (lane >> 5) * 8;
#pragma unroll
    for (int g = 0; g < 8; ++g) { b1v[g] = *(const u32x2*)(sp + g * 16); w2v[g] = *(const u32x2*)(sp + 128 + g * 16); }
    const int g = lane >> 3, g2 = g + 8 < ND_ ? g + 8 : ND_ - 1;
    const unsigned short* xp = (const unsigned short*)(lds + SMALL0 + 256) + (lane & 7) * 2 * ND_;
    xv[0] = xp[g]; xv[1] = xp[ND_ + g]; xv[2] = xp[g2]; xv[3] = xp[ND_ + g2];
  }
  phase<true>(c, 3, slot);
  STAMP_AT(7);

#pragma unroll
  for (int g = 0; g < 8; ++g) asm volatile("" : "+v"(b1v[g]), "+v"(w2v[g]));
  asm volatile("s_nop 7\n\ts_waitcnt vmcnt(0) lgkmcnt(0)\n\ts_barrier"
               : "+v"(xv[0]), "+v"(xv[1]), "+v"(xv[2]), "+v"(xv[3]), "+v"(wv_[0]), "+v"(wv_[1]), "+v"(bwv), "+v"(b2v), "+v"(epoch)
               :: "memory");
  f32x4 h[8], w2f[8];
  const unsigned s1 = (unsigned)(RED0 + (((w + 1) & 3) * 3 + 0) * 8192 + lane * 16);
  const unsigned s2 = (unsigned)(RED0 + (((w + 2) & 3) * 3 + 1) * 8192 + lane * 16);
  const unsigned s3 = (unsigned)(RED0 + (((w + 3) & 3) * 3 + 2) * 8192 + lane * 16);
#define SEND(g)                                                                                                   \
  writeAcc<32 + g * 4, g * 1024>(s1);                                                                             \
  h[g] = readAcc4<g>();                                                                                           \
  h[g].x += __uint_as_float(b1v[g].x << 16); h[g].y += __uint_as_float(b1v[g].x & 0xffff0000u);                   \
  asm volatile("" : "+v"(h[g]));                                                                                  \
  writeAcc<64 + g * 4, g * 1024>(s2);                                                                             \
  h[g].z += __uint_as_float(b1v[g].y << 16); h[g].w += __uint_as_float(b1v[g].y & 0xffff0000u);                   \
  w2f[g].x = __uint_as_float(w2v[g].x << 16); w2f[g].y = __uint_as_float(w2v[g].x & 0xffff0000u);                 \
  asm volatile("" : "+v"(h[g]), "+v"(w2f[g].x), "+v"(w2f[g].y));                                                  \
  writeAcc<96 + g * 4, g * 1024>(s3);                                                                             \
  w2f[g].z = __uint_as_float(w2v[g].y << 16); w2f[g].w = __uint_as_float(w2v[g].y & 0xffff0000u);                 \
  asm volatile("" : "+v"(w2f[g].z), "+v"(w2f[g].w));
  SEND(0) SEND(1) SEND(2) SEND(3) SEND(4) SEND(5) SEND(6) SEND(7)
#undef SEND
  float wide = __uint_as_float(xv[0] << 16) * __uint_as_float(wv_[0] << 16), wide1 = __uint_as_float(xv[1] << 16) * __uint_as_float(wv_[0] << 16);
  if ((lane >> 3) + 8 < ND_) {
    wide += __uint_as_float(xv[2] << 16) * __uint_as_float(wv_[1] << 16);
    wide1 += __uint_as_float(xv[3] << 16) * __uint_as_float(wv_[1] << 16);
  }
  wide = sumX32(sumX16(sumX8(wide))); wide1 = sumX32(sumX16(sumX8(wide1)));
  asm volatile("s_waitcnt lgkmcnt(0)\n\ts_barrier" : "+v"(wide), "+v"(wide1) :: "memory");
  STAMP_AT(8);
  f32x4 r[24];
#pragma unroll
  for (int g = 0; g < 8; ++g) {
    const unsigned char* p = lds + RED0 + w * 3 * 8192 + g * 1024 + lane * 16;
    r[g * 3] = *(const f32x4*)p; r[g * 3 + 1] = *(const f32x4*)(p + 8192); r[g * 3 + 2] = *(const f32x4*)(p + 16384);
  }
  f32x4 pacc = {0.f, 0.f, 0.f, 0.f};
#pragma unroll
  for (int g = 0; g < 8; ++g) {
    if ((g & 1) == 0)
      asm volatile("" : "+v"(r[g * 3]), "+v"(r[g * 3 + 1]), "+v"(r[g * 3 + 2]), "+v"(r[g * 3 + 3]), "+v"(r[g * 3 + 4]), "+v"(r[g * 3 + 5]) :: "memory");
    const f32x4 s = (h[g] + r[g * 3]) + (r[g * 3 + 1] + r[g * 3 + 2]);
    pacc.x = fmaf(fmaxf(s.x, 0.f), w2f[g].x, pacc.x); pacc.y = fmaf(fmaxf(s.y, 0.f), w2f[g].y, pacc.y);
    pacc.z = fmaf(fmaxf(s.z, 0.f), w2f[g].z, pacc.z); pacc.w = fmaf(fmaxf(s.w, 0.f), w2f[g].w, pacc.w);
  }
  const float part = sumX32((pacc.x + pacc.y) + (pacc.z + pacc.w));

  const unsigned tag = epoch + 1u;
  gu64* gran = (gu64*)(ws + WS_GRAN);
  if (lane < 32)
    __hip_atomic_store(gran + (size_t)((mt * 8 + nt) * 128 + w * 32 + lane), ((unsigned long long)tag << 32) | __float_as_uint(part),
                       __ATOMIC_RELAXED, __HIP_MEMORY_SCOPE_AGENT);
  STAMP_AT(9);
  if (w == 0) {
    const unsigned char* gp = ws + WS_GRAN + (size_t)((mt * 8 + (lane >> 3)) * 128 + nt * 16 + (lane & 7) * 2) * 8;
    u32x4 gv;
#pragma unroll 1
    for (unsigned spins = 0; spins < (1u << 22); ++spins) {
      asm volatile("global_load_dwordx4 %0, %1, off sc1\n\ts_waitcnt vmcnt(0)" : "=v"(gv) : "v"(gp) : "memory");
      if (__all(gv.y == tag && gv.w == tag)) break;
    }
    float d0 = __uint_as_float(gv.x), d1 = __uint_as_float(gv.z);
    d0 = sumX32(sumX16(sumX8(d0))); d1 = sumX32(sumX16(sumX8(d1)));
    if (lane < 8) {
      const float bwf = __uint_as_float(bwv << 16), b2f = __uint_as_float(b2v << 16);
      const float z0 = rbf(rbf(rbf(wide) + bwf) + rbf(rbf(d0) + b2f)), z1 = rbf(rbf(rbf(wide1) + bwf) + rbf(rbf(d1) + b2f));
      __hip_bfloat162 o;
      o.x = __float2bfloat16(1.f / (1.f + __expf(-z0)));
      o.y = __float2bfloat16(1.f / (1.f + __expf(-z1)));
      *(__hip_bfloat162*)(out + m0 + nt * 16 + lane * 2) = o;
    }
    if (nt == 0 && lane == 0) __hip_atomic_store((gu32*)(ws + mt * 128), tag, __ATOMIC_RELAXED, __HIP_MEMORY_SCOPE_AGENT);
  }
}

extern "C" void kernel_launch(void* const* d_in, const int* in_sizes, int n_in, void* d_out, int out_size, void* d_ws,
                              size_t ws_size, hipStream_t stream) {
  (void)in_sizes; (void)n_in; (void)out_size;
  if (ws_size < (size_t)WS_GRAN + 32 * 8 * 128 * 8) return;
  (void)hipFuncSetAttribute((const void*)wide_deep_kernel, hipFuncAttributeMaxDynamicSharedMemorySize, LDS_BYTES);
  wide_deep_kernel<<<256, 256, LDS_BYTES, stream>>>(
      (const int*)d_in[0], (const unsigned short*)d_in[1], (const unsigned short*)d_in[2], (const unsigned short*)d_in[3],
      (const unsigned short*)d_in[4], (const unsigned short*)d_in[5], (const unsigned short*)d_in[6],
      (const unsigned short*)d_in[7], (const unsigned short*)d_in[8], (__hip_bfloat16*)d_out, (unsigned char*)d_ws);
}
```

```cpp
#include <hip/hip_runtime.h>
#include <hip/hip_bf16.h>
#include <stdint.h>

typedef unsigned u32x4 __attribute__((ext_vector_type(4)));
typedef unsigned u32x2 __attribute__((ext_vector_type(2)));
typedef float f32x4 __attribute__((ext_vector_type(4)));
typedef __attribute__((address_space(1))) unsigned long long gu64;
typedef __attribute__((address_space(1))) unsigned gu32;

constexpr int B_ = 4096, F_ = 26, D_ = 128, V_ = 1000, ND_ = 13, H_ = 512;
constexpr int LDS_BYTES = 163840;
constexpr int RING0 = 131072;
constexpr int SMALL0 = 64512;
constexpr int RED0 = 65536;
constexpr int WS_GRAN = 4096;

#define WREC (F_ * D_ * H_ * 2)
#define TREC (V_ * D_ * 2)
#define ACLOB "a0","a1","a2","a3","a4","a5","a6","a7","a8","a9","a10","a11","a12","a13","a14","a15", \
  "a16","a17","a18","a19","a20","a21","a22","a23","a24","a25","a26","a27","a28","a29","a30","a31", \
  "a32","a33","a34","a35","a36","a37","a38","a39","a40","a41","a42","a43","a44","a45","a46","a47", \
  "a48","a49","a50","a51","a52","a53","a54","a55","a56","a57","a58","a59","a60","a61","a62","a63", \
  "a64","a65","a66","a67","a68","a69","a70","a71","a72","a73","a74","a75","a76","a77","a78","a79", \
  "a80","a81","a82","a83","a84","a85","a86","a87","a88","a89","a90","a91","a92","a93","a94","a95", \
  "a96","a97","a98","a99","a100","a101","a102","a103","a104","a105","a106","a107","a108","a109","a110","a111", \
  "a112","a113","a114","a115","a116","a117","a118","a119","a120","a121","a122","a123","a124","a125","a126","a127", \
  "a128","a129","a130","a131","a132","a133","a134","a135","a136","a137","a138","a139","a140","a141","a142","a143", \
  "a144","a145","a146","a147","a148","a149","a150","a151","a152","a153","a154","a155","a156","a157","a158","a159", \
  "a160","a161","a162","a163","a164","a165","a166","a167","a168","a169","a170","a171","a172","a173","a174","a175", \
  "a176","a177","a178","a179","a180","a181","a182","a183","a184","a185","a186","a187","a188","a189","a190","a191", \
  "a192","a193","a194","a195","a196","a197","a198","a199"

struct Ctx {
  unsigned addr[13][4];
  unsigned tb0, tb1;
  unsigned vW0, vW1;
  unsigned vT[4];
  __amdgpu_buffer_rsrc_t srdW, srdT;
  unsigned ring;
  unsigned wbase;
  unsigned tgrp;
};

__host__ __device__ constexpr int setOf(int j) { return j == 0 ? 2 : (j & 1); }
__host__ __device__ constexpr int regE(int set, int i) { return 128 + set * 24 + i * 4; }
__host__ __device__ constexpr int regW(int set, int nb) { return 128 + set * 24 + 16 + nb * 4; }
__host__ __device__ constexpr int regC(int i, int nb) { return (i * 2 + nb) * 16; }

#define AMP_A 15
#define AMP_B 3
#define RT(x) asm volatile("s_memrealtime %0\n\ts_waitcnt lgkmcnt(0)" : "=s"(x) :: "memory")
#define STAMP_AT(i) do { if constexpr ((i) == AMP_A) RT(tA); if constexpr ((i) == AMP_B) RT(tB); } while (0)
#define STAMP_RT(i) do {} while (0)
template <int N> __device__ __forceinline__ void waitV() { asm volatile("s_waitcnt vmcnt(%0)" ::"n"(N) : "memory"); }
template <int N> __device__ __forceinline__ void waitL() { asm volatile("s_waitcnt lgkmcnt(%0)" ::"n"(N) : "memory"); }
__device__ __forceinline__ void bar() { asm volatile("s_barrier" ::: "memory"); }

template <int R> __device__ __forceinline__ void readE(unsigned& a) {
  asm volatile("ds_read_b128 a[%c1:%c2], %0\n\tv_xor_b32 %0, 0x10000, %0" : "+v"(a) : "i"(R), "i"(R + 3) : "memory");
}
template <int R, int OFF> __device__ __forceinline__ void readW(unsigned a) {
  asm volatile("ds_read_b64_tr_b16 a[%c1:%c2], %0 offset:%c3\n\tds_read_b64_tr_b16 a[%c4:%c5], %0 offset:%c6"
               ::"v"(a), "i"(R), "i"(R + 1), "i"(OFF), "i"(R + 2), "i"(R + 3), "i"(OFF + 512) : "memory");
}
template <int C, int W, int E> __device__ __forceinline__ void mfma() {
  asm volatile("v_mfma_f32_32x32x16_bf16 a[%c0:%c1], a[%c2:%c3], a[%c4:%c5], a[%c0:%c1]"
               ::"i"(C), "i"(C + 15), "i"(W), "i"(W + 3), "i"(E), "i"(E + 3));
}
template <unsigned M, unsigned SO, int OFF>
__device__ __forceinline__ void dma1(unsigned m0a, unsigned voff, __amdgpu_buffer_rsrc_t srd, unsigned soa) {
  unsigned so;
  asm volatile("s_add_u32 m0, %1, %5\n\ts_add_u32 %0, %4, %6\n\tbuffer_load_dwordx4 %2, %3, %0 offen offset:%c7 lds"
               : "=&s"(so) : "s"(m0a), "v"(voff), "s"(srd), "s"(soa), "i"(M), "i"(SO), "i"(OFF) : "memory", "scc");
}

__host__ __device__ constexpr int nS(int j, bool last) { return (last || j < 0 || j > 10) ? 0 : (j == 10 ? 1 : 2 - (j & 1)); }
__host__ __device__ constexpr int firstS(int j) { int n = 0; for (int i = 0; i < j; ++i) n += nS(i, false); return n; }
__host__ __device__ constexpr int nB(int j, bool last) { return (!last || j <= 8) ? 2 : 0; }
__host__ __device__ constexpr int opsAll(int j, bool last) { return j < 0 ? 2 + nS(j + 13, false) : nB(j, last) + nS(j, last); }

template <int K> __device__ __forceinline__ void dmaT(const Ctx& c, unsigned m0a, unsigned soa) {
  constexpr int g = K >> 2, sub = K & 3;
  if constexpr (K == 15) m0a = c.tgrp == 3 * 4096u ? 130048u - 52224u : m0a;
  dma1<g * 16384u, g * 65536u, sub * 1024>(m0a, c.vT[sub], c.srdT, soa);
}

template <int J, bool LAST>
__device__ __forceinline__ void step(Ctx& c, unsigned wq, unsigned lb, unsigned tq, unsigned& slot) {
  constexpr int cur = setOf(J), JN = (J + 1) % 13, nxt = setOf(JN);
  constexpr bool hasNext = !(LAST && J == 12);
  constexpr int ns = nS(J, LAST), k0 = firstS(J);
  constexpr bool doW = nB(J, LAST) != 0;
  constexpr unsigned WO = (unsigned)((J + 4) % 13) * 131072u + ((J + 4) >= 13 ? 32768u : 0u);
  const unsigned slotN = (slot + 2048) & 8191;
  if constexpr (J == 12 && !LAST) { waitV<4>(); waitL<0>(); bar(); }
  else { if constexpr (hasNext) waitV<opsAll(J - 2, LAST) + opsAll(J - 1, LAST)>(); waitL<3>(); }
  const unsigned t0 = c.tb0 + slotN, t1 = c.tb1 + slotN, ms = c.ring + slot;
  mfma<regC(0, 0), regW(cur, 0), regE(cur, 0)>();
  if constexpr (hasNext) readW<regW(nxt, 0), 0>(t0);
  mfma<regC(0, 1), regW(cur, 1), regE(cur, 0)>();
  if constexpr (hasNext) readW<regW(nxt, 1), 0>(t1);
  if constexpr (hasNext) waitL<6>(); else waitL<2>();
  mfma<regC(1, 0), regW(cur, 0), regE(cur, 1)>();
  if constexpr (hasNext) readE<regE(nxt, 0)>(c.addr[JN][0]);
  if constexpr (ns >= 1) dmaT<k0>(c, lb, tq);
  mfma<regC(1, 1), regW(cur, 1), regE(cur, 1)>();
  if constexpr (doW) dma1<0, WO, 0>(ms, c.vW0, c.srdW, wq);
  if constexpr (hasNext) waitL<6>(); else waitL<1>();
  mfma<regC(2, 0), regW(cur, 0), regE(cur, 2)>();
  if constexpr (hasNext) readE<regE(nxt, 1)>(c.addr[JN][1]);
  if constexpr (ns >= 2) dmaT<k0 + 1>(c, lb, tq);
  mfma<regC(2, 1), regW(cur, 1), regE(cur, 2)>();
  if constexpr (doW) dma1<0, WO, 1024>(ms, c.vW1, c.srdW, wq);
  if constexpr (hasNext) waitL<6>(); else waitL<0>();
  mfma<regC(3, 0), regW(cur, 0), regE(cur, 3)>();
  if constexpr (hasNext) readE<regE(nxt, 2)>(c.addr[JN][2]);
  mfma<regC(3, 1), regW(cur, 1), regE(cur, 3)>();
  if constexpr (hasNext) readE<regE(nxt, 3)>(c.addr[JN][3]);
  slot = slotN;
}

template <bool LAST> __device__ __forceinline__ void phase(Ctx& c, int q, unsigned& slot) {
  const unsigned wq = c.wbase + (unsigned)q * 32768u, lb = (unsigned)(((q + 1) & 1) << 16) + c.tgrp,
                 tq = c.tgrp * 4u + (unsigned)(q + 1) * 64u;
  step<0, LAST>(c, wq, lb, tq, slot); step<1, LAST>(c, wq, lb, tq, slot); step<2, LAST>(c, wq, lb, tq, slot); step<3, LAST>(c, wq, lb, tq, slot);
  step<4, LAST>(c, wq, lb, tq, slot); step<5, LAST>(c, wq, lb, tq, slot); step<6, LAST>(c, wq, lb, tq, slot); step<7, LAST>(c, wq, lb, tq, slot);
  step<8, LAST>(c, wq, lb, tq, slot); step<9, LAST>(c, wq, lb, tq, slot); step<10, LAST>(c, wq, lb, tq, slot); step<11, LAST>(c, wq, lb, tq, slot);
  step<12, LAST>(c, wq, lb, tq, slot);
}

template <int R> __device__ __forceinline__ void zero1() { asm volatile("v_accvgpr_write_b32 a[%c0], 0" ::"i"(R)); }
template <int R, int N> struct Zero { static __device__ __forceinline__ void run() { zero1<R>(); Zero<R + 1, N - 1>::run(); } };
template <int R> struct Zero<R, 0> { static __device__ __forceinline__ void run() {} };

template <int R, int OFF> __device__ __forceinline__ void writeAcc(unsigned a) {
  asm volatile("ds_write_b128 %0, a[%c1:%c2] offset:%c3" ::"v"(a), "i"(R), "i"(R + 3), "i"(OFF) : "memory");
}
template <int R> __device__ __forceinline__ float readAcc() {
  float v; asm volatile("v_accvgpr_read_b32 %0, a[%c1]" : "=v"(v) : "i"(R)); return v;
}
template <int G> __device__ __forceinline__ f32x4 readAcc4() {
  f32x4 v; v.x = readAcc<G * 4>(); v.y = readAcc<G * 4 + 1>(); v.z = readAcc<G * 4 + 2>(); v.w = readAcc<G * 4 + 3>(); return v;
}

__device__ __forceinline__ float sumX8(float x) {
  return x + __int_as_float(__builtin_amdgcn_update_dpp(0, __float_as_int(x), 0x128, 0xf, 0xf, true));
}
__device__ __forceinline__ float sumX16(float x) {
  const auto r = __builtin_amdgcn_permlane16_swap(__float_as_uint(x), __float_as_uint(x), false, false);
  return __uint_as_float(r[0]) + __uint_as_float(r[1]);
}
__device__ __forceinline__ float sumX32(float x) {
  const auto r = __builtin_amdgcn_permlane32_swap(__float_as_uint(x), __float_as_uint(x), false, false);
  return __uint_as_float(r[0]) + __uint_as_float(r[1]);
}
__device__ __forceinline__ float rbf(float x) { return __bfloat162float(__float2bfloat16(x)); }

__global__ __launch_bounds__(256, 1) void wide_deep_kernel(
    const int* __restrict__ idx, const unsigned short* __restrict__ table, const unsigned short* __restrict__ xw,
    const unsigned short* __restrict__ ww, const unsigned short* __restrict__ bw, const unsigned short* __restrict__ W1,
    const unsigned short* __restrict__ b1, const unsigned short* __restrict__ W2, const unsigned short* __restrict__ b2,
    __hip_bfloat16* __restrict__ out, unsigned char* ws) {
  extern __shared__ __attribute__((aligned(16))) unsigned char lds[];
  const int tid = threadIdx.x, lane = tid & 63;
  const int w = __builtin_amdgcn_readfirstlane(tid >> 6);
  const int nt = blockIdx.x & 7, mt = blockIdx.x >> 3;
  const int n0 = nt * 64, m0 = mt * 128;
  const int fh = w >> 1, ks = w & 1;

  unsigned long long tA = 0, tB = 0;
  STAMP_AT(0);

  asm volatile("" ::"s"(idx), "s"(table), "s"(xw), "s"(ww), "s"(bw), "s"(W1), "s"(b1), "s"(W2), "s"(b2), "s"(out), "s"(ws));
  unsigned epoch, wv_[2] = {0, 0}, bwv = 0, b2v = 0;
#define LDH(dst, ptr) asm volatile("global_load_ushort %0, %1, off" : "=v"(dst) : "v"(ptr) : "memory")
  asm volatile("global_load_dword %0, %1, off sc1" : "=v"(epoch) : "v"(ws + mt * 128) : "memory");
  if (w == 0) {
    const int g = lane >> 3, g2 = g + 8 < ND_ ? g + 8 : ND_ - 1;
    LDH(wv_[0], ww + g); LDH(wv_[1], ww + g2); LDH(bwv, bw); LDH(b2v, b2);
  }
#undef LDH
  Ctx c;
  const __amdgpu_buffer_rsrc_t srdI = __builtin_amdgcn_make_buffer_rsrc((void*)idx, (short)0, B_ * F_ * 4, 0x00020000);
  {
    const unsigned vI = (unsigned)lane * 16u, p3 = w == 0 ? 12u : (unsigned)w + 8u;
    dma1<65536u, 0, 0>((unsigned)w * 1024u, vI, srdI, (unsigned)(m0 * F_ * 4 + w * 1024));
    dma1<65536u + 4096u, 4096u, 0>((unsigned)w * 1024u, vI, srdI, (unsigned)(m0 * F_ * 4 + w * 1024));
    dma1<65536u, 0, 0>(p3 * 1024u, vI, srdI, (unsigned)(m0 * F_ * 4) + p3 * 1024u);
    dma1<65536u, 0, 0>((w == 0 ? 8u : 12u) * 1024u, vI, srdI, (unsigned)(m0 * F_ * 4) + (w == 0 ? 8u : 12u) * 1024u);
  }
  unsigned warm = 0, warm2 = 0;
  {
    const int ln = mt * 64 + (tid >> 2);
    const unsigned short* p2 = table + (size_t)(ln < 2000 ? ln : 1999) * 64;
    const int t = tid < 208 ? tid : 207;
    const unsigned short* p = W1 + (size_t)(mt * 104 + (t >> 1)) * H_ + n0 + (t & 1) * 32;
    asm volatile("global_load_dword %0, %2, off\n\tglobal_load_dword %1, %3, off"
                 : "=&v"(warm2), "=&v"(warm) : "v"(p2), "v"(p) : "memory");
  }
  c.srdW = __builtin_amdgcn_make_buffer_rsrc((void*)W1, (short)0, WREC, 0x00020000);
  c.srdT = __builtin_amdgcn_make_buffer_rsrc((void*)table, (short)0, TREC, 0x00020000);
  c.ring = RING0 + w * 8192;
  c.wbase = (unsigned)(fh * 13 * 128 + ks * 16) * 1024u;
  c.tgrp = (unsigned)w * 4096u;
  c.vT[0] = (unsigned)(lane >> 2) * 256u + (unsigned)(((lane & 3) ^ ((lane >> 4) & 3)) * 16);
  c.vT[1] = c.vT[0] + 3072u; c.vT[2] = c.vT[0] + 6144u; c.vT[3] = c.vT[0] + 9216u;
  c.vW0 = (unsigned)(lane >> 3) * 1024u + (unsigned)(((lane & 7) ^ (((lane >> 4) & 1) << 2)) * 16) + (unsigned)n0 * 2u;
  c.vW1 = c.vW0 + 7168u;
  {
    const int qq = (lane & 15) >> 2, p = lane & 3, swz = (qq >> 1) & 1;
    const unsigned base = c.ring + (unsigned)((8 * (lane >> 5) + qq) * 128 + ((lane >> 4) & 1) * 32 + p * 8);
    c.tb0 = base + (unsigned)(swz * 64);
    c.tb1 = base + (unsigned)((swz ^ 1) * 64);
  }
  STAMP_AT(13);
  asm volatile("" ::: ACLOB);
  const unsigned tq0 = c.tgrp * 4u;
  dmaT<0>(c, c.tgrp, tq0); dmaT<1>(c, c.tgrp, tq0); Zero<0, 16>::run();
  dma1<0, 0, 0>(c.ring, c.vW0, c.srdW, c.wbase); dma1<0, 0, 1024>(c.ring, c.vW1, c.srdW, c.wbase); Zero<16, 16>::run();
  dmaT<2>(c, c.tgrp, tq0); dmaT<3>(c, c.tgrp, tq0); Zero<32, 16>::run();
  dmaT<4>(c, c.tgrp, tq0); dmaT<5>(c, c.tgrp, tq0); Zero<48, 16>::run();
  dmaT<6>(c, c.tgrp, tq0); dmaT<7>(c, c.tgrp, tq0); Zero<64, 16>::run();
  STAMP_AT(14);
  waitV<12>();
  bar();
  STAMP_AT(15);
  unsigned warm3 = 0;
  asm volatile("global_load_dword %0, %1, off" : "=v"(warm3) : "v"(out + m0 + nt * 16) : "memory");
  {
    const unsigned cs = (unsigned)(ks * 2 + (lane >> 5)) << 4;
#define ADDR_BLOCK(i)                                                                          \
    {                                                                                          \
      const int row = ((w + i) & 3) * 32 + (lane & 31);                                        \
      const int* ip = (const int*)(lds + 65536 + row * (F_ * 4) + fh * 52);                    \
      unsigned v[13];                                                                          \
      _Pragma("unroll") for (int j = 0; j < 13; ++j) v[j] = (unsigned)ip[j];                   \
      dmaT<8 + 2 * i>(c, c.tgrp, tq0); dmaT<9 + 2 * i>(c, c.tgrp, tq0);                  \
      Zero<80 + 12 * i, 12>::run();                                                            \
      _Pragma("unroll") for (int j = 0; j < 13; ++j)                                           \
        c.addr[j][i] = (v[j] << 6) | (((v[j] << 2) & 0x30u) ^ cs);                             \
    }
    ADDR_BLOCK(0) ADDR_BLOCK(1) ADDR_BLOCK(2) ADDR_BLOCK(3)
#undef ADDR_BLOCK
  }
  STAMP_AT(16);
  dma1<2048, 131072, 0>(c.ring, c.vW0, c.srdW, c.wbase); dma1<2048, 131072, 1024>(c.ring, c.vW1, c.srdW, c.wbase);
  {
    const unsigned char* src = lane >= 16 && lane < 42 ? (const unsigned char*)(xw + (size_t)(m0 + nt * 16) * ND_) + (lane - 16) * 16
                               : (const unsigned char*)((lane >> 3) == 1 ? W2 : b1) + n0 * 2 + (lane & 7) * 16;
    asm volatile("s_mov_b32 m0, %0\n\ts_nop 0\n\tglobal_load_lds_dwordx4 %1, off" ::"s"(SMALL0), "v"(src) : "memory");
  }
  dma1<4096, 2 * 131072, 0>(c.ring, c.vW0, c.srdW, c.wbase); dma1<4096, 2 * 131072, 1024>(c.ring, c.vW1, c.srdW, c.wbase);
  dma1<6144, 3 * 131072, 0>(c.ring, c.vW0, c.srdW, c.wbase); dma1<6144, 3 * 131072, 1024>(c.ring, c.vW1, c.srdW, c.wbase);
  STAMP_AT(1);
  waitL<0>();
  waitV<2 + 1 + 4>();
  asm volatile("" ::"v"(warm), "v"(warm2), "v"(warm3));
  bar();
  STAMP_AT(3);
  readW<regW(2, 0), 0>(c.tb0);
  readW<regW(2, 1), 0>(c.tb1);
  readE<regE(2, 0)>(c.addr[0][0]); readE<regE(2, 1)>(c.addr[0][1]);
  readE<regE(2, 2)>(c.addr[0][2]); readE<regE(2, 3)>(c.addr[0][3]);

  unsigned slot = 0;
#pragma nounroll
  for (int q = 0; q < 3; ++q) {
    phase<false>(c, q, slot);
    if (q == 0) STAMP_AT(4);
  }
  u32x2 b1v[8], w2v[8];
  unsigned xv[4];
  {
    const unsigned char* sp = lds + SMALL0 + (lane >> 5) * 8;
#pragma unroll
    for (int g = 0; g < 8; ++g) { b1v[g] = *(const u32x2*)(sp + g * 16); w2v[g] = *(const u32x2*)(sp + 128 + g * 16); }
    const int g = lane >> 3, g2 = g + 8 < ND_ ? g + 8 : ND_ - 1;
    const unsigned short* xp = (const unsigned short*)(lds + SMALL0 + 256) + (lane & 7) * 2 * ND_;
    xv[0] = xp[g]; xv[1] = xp[ND_ + g]; xv[2] = xp[g2]; xv[3] = xp[ND_ + g2];
  }
  phase<true>(c, 3, slot);
  STAMP_AT(7);

#pragma unroll
  for (int g = 0; g < 8; ++g) asm volatile("" : "+v"(b1v[g]), "+v"(w2v[g]));
  asm volatile("s_nop 7\n\ts_waitcnt vmcnt(0) lgkmcnt(0)\n\ts_barrier"
               : "+v"(xv[0]), "+v"(xv[1]), "+v"(xv[2]), "+v"(xv[3]), "+v"(wv_[0]), "+v"(wv_[1]), "+v"(bwv), "+v"(b2v), "+v"(epoch)
               :: "memory");
  f32x4 h[8], w2f[8];
  const unsigned s1 = (unsigned)(RED0 + (((w + 1) & 3) * 3 + 0) * 8192 + lane * 16);
  const unsigned s2 = (unsigned)(RED0 + (((w + 2) & 3) * 3 + 1) * 8192 + lane * 16);
  const unsigned s3 = (unsigned)(RED0 + (((w + 3) & 3) * 3 + 2) * 8192 + lane * 16);
#define SEND(g)                                                                                                   \
  writeAcc<32 + g * 4, g * 1024>(s1);                                                                             \
  h[g] = readAcc4<g>();                                                                                           \
  h[g].x += __uint_as_float(b1v[g].x << 16); h[g].y += __uint_as_float(b1v[g].x & 0xffff0000u);                   \
  asm volatile("" : "+v"(h[g]));                                                                                  \
  writeAcc<64 + g * 4, g * 1024>(s2);                                                                             \
  h[g].z += __uint_as_float(b1v[g].y << 16); h[g].w += __uint_as_float(b1v[g].y & 0xffff0000u);                   \
  w2f[g].x = __uint_as_float(w2v[g].x << 16); w2f[g].y = __uint_as_float(w2v[g].x & 0xffff0000u);                 \
  asm volatile("" : "+v"(h[g]), "+v"(w2f[g].x), "+v"(w2f[g].y));                                                  \
  writeAcc<96 + g * 4, g * 1024>(s3);                                                                             \
  w2f[g].z = __uint_as_float(w2v[g].y << 16); w2f[g].w = __uint_as_float(w2v[g].y & 0xffff0000u);                 \
  asm volatile("" : "+v"(w2f[g].z), "+v"(w2f[g].w));
  SEND(0) SEND(1) SEND(2) SEND(3) SEND(4) SEND(5) SEND(6) SEND(7)
#undef SEND
  float wide = __uint_as_float(xv[0] << 16) * __uint_as_float(wv_[0] << 16), wide1 = __uint_as_float(xv[1] << 16) * __uint_as_float(wv_[0] << 16);
  if ((lane >> 3) + 8 < ND_) {
    wide += __uint_as_float(xv[2] << 16) * __uint_as_float(wv_[1] << 16);
    wide1 += __uint_as_float(xv[3] << 16) * __uint_as_float(wv_[1] << 16);
  }
  wide = sumX32(sumX16(sumX8(wide))); wide1 = sumX32(sumX16(sumX8(wide1)));
  asm volatile("s_waitcnt lgkmcnt(0)\n\ts_barrier" : "+v"(wide), "+v"(wide1) :: "memory");
  STAMP_AT(8);
  f32x4 r[24];
#pragma unroll
  for (int g = 0; g < 8; ++g) {
    const unsigned char* p = lds + RED0 + w * 3 * 8192 + g * 1024 + lane * 16;
    r[g * 3] = *(const f32x4*)p; r[g * 3 + 1] = *(const f32x4*)(p + 8192); r[g * 3 + 2] = *(const f32x4*)(p + 16384);
  }
  f32x4 pacc = {0.f, 0.f, 0.f, 0.f};
#pragma unroll
  for (int g = 0; g < 8; ++g) {
    if ((g & 1) == 0)
      asm volatile("" : "+v"(r[g * 3]), "+v"(r[g * 3 + 1]), "+v"(r[g * 3 + 2]), "+v"(r[g * 3 + 3]), "+v"(r[g * 3 + 4]), "+v"(r[g * 3 + 5]) :: "memory");
    const f32x4 s = (h[g] + r[g * 3]) + (r[g * 3 + 1] + r[g * 3 + 2]);
    pacc.x = fmaf(fmaxf(s.x, 0.f), w2f[g].x, pacc.x); pacc.y = fmaf(fmaxf(s.y, 0.f), w2f[g].y, pacc.y);
    pacc.z = fmaf(fmaxf(s.z, 0.f), w2f[g].z, pacc.z); pacc.w = fmaf(fmaxf(s.w, 0.f), w2f[g].w, pacc.w);
  }
  const float part = sumX32((pacc.x + pacc.y) + (pacc.z + pacc.w));

  const unsigned tag = epoch + 1u;
  gu64* gran = (gu64*)(ws + WS_GRAN);
  if (lane < 32)
    __hip_atomic_store(gran + (size_t)((mt * 8 + nt) * 128 + w * 32 + lane), ((unsigned long long)tag << 32) | __float_as_uint(part),
                       __ATOMIC_RELAXED, __HIP_MEMORY_SCOPE_AGENT);
  STAMP_AT(9);
  if (w == 0) {
    const unsigned char* gp = ws + WS_GRAN + (size_t)((mt * 8 + (lane >> 3)) * 128 + nt * 16 + (lane & 7) * 2) * 8;
    u32x4 gv;
#pragma unroll 1
    for (unsigned spins = 0; spins < (1u << 22); ++spins) {
      asm volatile("global_load_dwordx4 %0, %1, off sc1\n\ts_waitcnt vmcnt(0)" : "=v"(gv) : "v"(gp) : "memory");
      if (__all(gv.y == tag && gv.w == tag)) break;
    }
    float d0 = __uint_as_float(gv.x), d1 = __uint_as_float(gv.z);
    d0 = sumX32(sumX16(sumX8(d0))); d1 = sumX32(sumX16(sumX8(d1)));
    if (lane < 8) {
      const float bwf = __uint_as_float(bwv << 16), b2f = __uint_as_float(b2v << 16);
      const float z0 = rbf(rbf(rbf(wide) + bwf) + rbf(rbf(d0) + b2f)), z1 = rbf(rbf(rbf(wide1) + bwf) + rbf(rbf(d1) + b2f));
      __hip_bfloat162 o;
      o.x = __float2bfloat16(1.f / (1.f + __expf(-z0)));
      o.y = __float2bfloat16(1.f / (1.f + __expf(-z1)));
      *(__hip_bfloat162*)(out + m0 + nt * 16 + lane * 2) = o;
    }
    STAMP_AT(10);
    {
      unsigned long long d = tB - tA, t0, t1; if (d > 3000) d = 3000;
      RT(t0);
      do { RT(t1); } while (t1 - t0 < d * 10);
    }
    if (nt == 0 && lane == 0) __hip_atomic_store((gu32*)(ws + mt * 128), tag, __ATOMIC_RELAXED, __HIP_MEMORY_SCOPE_AGENT);
  }
}

extern "C" void kernel_launch(void* const* d_in, const int* in_sizes, int n_in, void* d_out, int out_size, void* d_ws,
                              size_t ws_size, hipStream_t stream) {
  (void)in_sizes; (void)n_in; (void)out_size;
  if (ws_size < (size_t)WS_GRAN + 32 * 8 * 128 * 8) return;
  (void)hipFuncSetAttribute((const void*)wide_deep_kernel, hipFuncAttributeMaxDynamicSharedMemorySize, LDS_BYTES);
  wide_deep_kernel<<<256, 256, LDS_BYTES, stream>>>(
      (const int*)d_in[0], (const unsigned short*)d_in[1], (const unsigned short*)d_in[2], (const unsigned short*)d_in[3],
      (const unsigned short*)d_in[4], (const unsigned short*)d_in[5], (const unsigned short*)d_in[6],
      (const unsigned short*)d_in[7], (const unsigned short*)d_in[8], (__hip_bfloat16*)d_out, (unsigned char*)d_ws);
}
```

```cpp
#include <hip/hip_runtime.h>
#include <hip/hip_bf16.h>
#include <stdint.h>

typedef unsigned u32x4 __attribute__((ext_vector_type(4)));
typedef unsigned u32x2 __attribute__((ext_vector_type(2)));
typedef float f32x4 __attribute__((ext_vector_type(4)));
typedef __attribute__((address_space(1))) unsigned long long gu64;
typedef __attribute__((address_space(1))) unsigned gu32;

constexpr int B_ = 4096, F_ = 26, D_ = 128, V_ = 1000, ND_ = 13, H_ = 512;
constexpr int LDS_BYTES = 163840;
constexpr int RING0 = 131072;
constexpr int SMALL0 = 64512;
constexpr int RED0 = 65536;
constexpr int WS_GRAN = 4096;

#define WREC (F_ * D_ * H_ * 2)
#define TREC (V_ * D_ * 2)
#define ACLOB "a0","a1","a2","a3","a4","a5","a6","a7","a8","a9","a10","a11","a12","a13","a14","a15", \
  "a16","a17","a18","a19","a20","a21","a22","a23","a24","a25","a26","a27","a28","a29","a30","a31", \
  "a32","a33","a34","a35","a36","a37","a38","a39","a40","a41","a42","a43","a44","a45","a46","a47", \
  "a48","a49","a50","a51","a52","a53","a54","a55","a56","a57","a58","a59","a60","a61","a62","a63", \
  "a64","a65","a66","a67","a68","a69","a70","a71","a72","a73","a74","a75","a76","a77","a78","a79", \
  "a80","a81","a82","a83","a84","a85","a86","a87","a88","a89","a90","a91","a92","a93","a94","a95", \
  "a96","a97","a98","a99","a100","a101","a102","a103","a104","a105","a106","a107","a108","a109","a110","a111", \
  "a112","a113","a114","a115","a116","a117","a118","a119","a120","a121","a122","a123","a124","a125","a126","a127", \
  "a128","a129","a130","a131","a132","a133","a134","a135","a136","a137","a138","a139","a140","a141","a142","a143", \
  "a144","a145","a146","a147","a148","a149","a150","a151","a152","a153","a154","a155","a156","a157","a158","a159", \
  "a160","a161","a162","a163","a164","a165","a166","a167","a168","a169","a170","a171","a172","a173","a174","a175", \
  "a176","a177","a178","a179","a180","a181","a182","a183","a184","a185","a186","a187","a188","a189","a190","a191", \
  "a192","a193","a194","a195","a196","a197","a198","a199"

struct Ctx {
  unsigned addr[13][4];
  unsigned tb0, tb1;
  unsigned vW0, vW1;
  unsigned vT[4];
  __amdgpu_buffer_rsrc_t srdW, srdT;
  unsigned ring;
  unsigned wbase;
  unsigned tgrp;
};

__host__ __device__ constexpr int setOf(int j) { return j == 0 ? 2 : (j & 1); }
__host__ __device__ constexpr int regE(int set, int i) { return 128 + set * 24 + i * 4; }
__host__ __device__ constexpr int regW(int set, int nb) { return 128 + set * 24 + 16 + nb * 4; }
__host__ __device__ constexpr int regC(int i, int nb) { return (i * 2 + nb) * 16; }

#define STAMP_AT(i) do {} while (0)
#define STAMP_RT(i) do {} while (0)
template <int N> __device__ __forceinline__ void waitV() { asm volatile("s_waitcnt vmcnt(%0)" ::"n"(N) : "memory"); }
template <int N> __device__ __forceinline__ void waitL() { asm volatile("s_waitcnt lgkmcnt(%0)" ::"n"(N) : "memory"); }
__device__ __forceinline__ void bar() { asm volatile("s_barrier" ::: "memory"); }

template <int R> __device__ __forceinline__ void readE(unsigned& a) {
  asm volatile("ds_read_b128 a[%c1:%c2], %0\n\tv_xor_b32 %0, 0x10000, %0" : "+v"(a) : "i"(R), "i"(R + 3) : "memory");
}
template <int R, int OFF> __device__ __forceinline__ void readW(unsigned a) {
  asm volatile("ds_read_b64_tr_b16 a[%c1:%c2], %0 offset:%c3\n\tds_read_b64_tr_b16 a[%c4:%c5], %0 offset:%c6"
               ::"v"(a), "i"(R), "i"(R + 1), "i"(OFF), "i"(R + 2), "i"(R + 3), "i"(OFF + 512) : "memory");
}
template <int C, int W, int E> __device__ __forceinline__ void mfma() {
  asm volatile("v_mfma_f32_32x32x16_bf16 a[%c0:%c1], a[%c2:%c3], a[%c4:%c5], a[%c0:%c1]"
               ::"i"(C), "i"(C + 15), "i"(W), "i"(W + 3), "i"(E), "i"(E + 3));
}
template <unsigned M, unsigned SO, int OFF>
__device__ __forceinline__ void dma1(unsigned m0a, unsigned voff, __amdgpu_buffer_rsrc_t srd, unsigned soa) {
  unsigned so;
  asm volatile("s_add_u32 m0, %1, %5\n\ts_add_u32 %0, %4, %6\n\tbuffer_load_dwordx4 %2, %3, %0 offen offset:%c7 lds"
               : "=&s"(so) : "s"(m0a), "v"(voff), "s"(srd), "s"(soa), "i"(M), "i"(SO), "i"(OFF) : "memory", "scc");
}

__host__ __device__ constexpr int nS(int j, bool last) { return (last || j < 0 || j > 10) ? 0 : (j == 10 ? 1 : 2 - (j & 1)); }
__host__ __device__ constexpr int firstS(int j) { int n = 0; for (int i = 0; i < j; ++i) n += nS(i, false); return n; }
__host__ __device__ constexpr int nB(int j, bool last) { return (!last || j <= 8) ? 2 : 0; }
__host__ __device__ constexpr int opsAll(int j, bool last) { return j < 0 ? 2 + nS(j + 13, false) : nB(j, last) + nS(j, last); }

template <int K> __device__ __forceinline__ void dmaT(const Ctx& c, unsigned m0a, unsigned soa) {
  constexpr int g = K >> 2, sub = K & 3;
  if constexpr (K == 15) m0a = c.tgrp == 3 * 4096u ? 130048u - 52224u : m0a;
  dma1<g * 16384u, g * 65536u, sub * 1024>(m0a, c.vT[sub], c.srdT, soa);
}

template <int J, bool LAST>
__device__ __forceinline__ void step(Ctx& c, unsigned wq, unsigned lb, unsigned tq, unsigned& slot) {
  constexpr int cur = setOf(J), JN = (J + 1) % 13, nxt = setOf(JN);
  constexpr bool hasNext = !(LAST && J == 12);
  constexpr int ns = nS(J, LAST), k0 = firstS(J);
  constexpr bool doW = nB(J, LAST) != 0;
  constexpr unsigned WO = (unsigned)((J + 4) % 13) * 131072u + ((J + 4) >= 13 ? 32768u : 0u);
  const unsigned slotN = (slot + 2048) & 8191;
  if constexpr (J == 12 && !LAST) { waitV<4>(); waitL<0>(); bar(); }
  else { if constexpr (hasNext) waitV<opsAll(J - 2, LAST) + opsAll(J - 1, LAST)>(); waitL<3>(); }
  const unsigned t0 = c.tb0 + slotN, t1 = c.tb1 + slotN, ms = c.ring + slot;
  mfma<regC(0, 0), regW(cur, 0), regE(cur, 0)>();
  if constexpr (hasNext) readW<regW(nxt, 0), 0>(t0);
  mfma<regC(0, 1), regW(cur, 1), regE(cur, 0)>();
  if constexpr (hasNext) readW<regW(nxt, 1), 0>(t1);
  if constexpr (hasNext) waitL<6>(); else waitL<2>();
  mfma<regC(1, 0), regW(cur, 0), regE(cur, 1)>();
  if constexpr (hasNext) readE<regE(nxt, 0)>(c.addr[JN][0]);
  if constexpr (ns >= 1) dmaT<k0>(c, lb, tq);
  mfma<regC(1, 1), regW(cur, 1), regE(cur, 1)>();
  if constexpr (doW) dma1<0, WO, 0>(ms, c.vW0, c.srdW, wq);
  if constexpr (hasNext) waitL<6>(); else waitL<1>();
  mfma<regC(2, 0), regW(cur, 0), regE(cur, 2)>();
  if constexpr (hasNext) readE<regE(nxt, 1)>(c.addr[JN][1]);
  if constexpr (ns >= 2) dmaT<k0 + 1>(c, lb, tq);
  mfma<regC(2, 1), regW(cur, 1), regE(cur, 2)>();
  if constexpr (doW) dma1<0, WO, 1024>(ms, c.vW1, c.srdW, wq);
  if constexpr (hasNext) waitL<6>(); else waitL<0>();
  mfma<regC(3, 0), regW(cur, 0), regE(cur, 3)>();
  if constexpr (hasNext) readE<regE(nxt, 2)>(c.addr[JN][2]);
  mfma<regC(3, 1), regW(cur, 1), regE(cur, 3)>();
  if constexpr (hasNext) readE<regE(nxt, 3)>(c.addr[JN][3]);
  slot = slotN;
}

template <bool LAST> __device__ __forceinline__ void phase(Ctx& c, int q, unsigned& slot) {
  const unsigned wq = c.wbase + (unsigned)q * 32768u, lb = (unsigned)(((q + 1) & 1) << 16) + c.tgrp,
                 tq = c.tgrp * 4u + (unsigned)(q + 1) * 64u;
  step<0, LAST>(c, wq, lb, tq, slot); step<1, LAST>(c, wq, lb, tq, slot); step<2, LAST>(c, wq, lb, tq, slot); step<3, LAST>(c, wq, lb, tq, slot);
  step<4, LAST>(c, wq, lb, tq, slot); step<5, LAST>(c, wq, lb, tq, slot); step<6, LAST>(c, wq, lb, tq, slot); step<7, LAST>(c, wq, lb, tq, slot);
  step<8, LAST>(c, wq, lb, tq, slot); step<9, LAST>(c, wq, lb, tq, slot); step<10, LAST>(c, wq, lb, tq, slot); step<11, LAST>(c, wq, lb, tq, slot);
  step<12, LAST>(c, wq, lb, tq, slot);
}

template <int R> __device__ __forceinline__ void zero1() { asm volatile("v_accvgpr_write_b32 a[%c0], 0" ::"i"(R)); }
template <int R, int N> struct Zero { static __device__ __forceinline__ void run() { zero1<R>(); Zero<R + 1, N - 1>::run(); } };
template <int R> struct Zero<R, 0> { static __device__ __forceinline__ void run() {} };

template <int R, int OFF> __device__ __forceinline__ void writeAcc(unsigned a) {
  asm volatile("ds_write_b128 %0, a[%c1:%c2] offset:%c3" ::"v"(a), "i"(R), "i"(R + 3), "i"(OFF) : "memory");
}
template <int R> __device__ __forceinline__ float readAcc() {
  float v; asm volatile("v_accvgpr_read_b32 %0, a[%c1]" : "=v"(v) : "i"(R)); return v;
}
template <int G> __device__ __forceinline__ f32x4 readAcc4() {
  f32x4 v; v.x = readAcc<G * 4>(); v.y = readAcc<G * 4 + 1>(); v.z = readAcc<G * 4 + 2>(); v.w = readAcc<G * 4 + 3>(); return v;
}

__device__ __forceinline__ float sumX8(float x) {
  return x + __int_as_float(__builtin_amdgcn_update_dpp(0, __float_as_int(x), 0x128, 0xf, 0xf, true));
}
__device__ __forceinline__ float sumX16(float x) {
  const auto r = __builtin_amdgcn_permlane16_swap(__float_as_uint(x), __float_as_uint(x), false, false);
  return __uint_as_float(r[0]) + __uint_as_float(r[1]);
}
__device__ __forceinline__ float sumX32(float x) {
  const auto r = __builtin_amdgcn_permlane32_swap(__float_as_uint(x), __float_as_uint(x), false, false);
  return __uint_as_float(r[0]) + __uint_as_float(r[1]);
}
__device__ __forceinline__ float rbf(float x) { return __bfloat162float(__float2bfloat16(x)); }

__global__ __launch_bounds__(256, 1) void wide_deep_kernel(
    const int* __restrict__ idx, const unsigned short* __restrict__ table, const unsigned short* __restrict__ xw,
    const unsigned short* __restrict__ ww, const unsigned short* __restrict__ bw, const unsigned short* __restrict__ W1,
    const unsigned short* __restrict__ b1, const unsigned short* __restrict__ W2, const unsigned short* __restrict__ b2,
    __hip_bfloat16* __restrict__ out, unsigned char* ws) {
  extern __shared__ __attribute__((aligned(16))) unsigned char lds[];
  const int tid = threadIdx.x, lane = tid & 63;
  const int w = __builtin_amdgcn_readfirstlane(tid >> 6);
  const int nt = blockIdx.x & 7, mt = blockIdx.x >> 3;
  const int n0 = nt * 64, m0 = mt * 128;
  const int fh = w >> 1, ks = w & 1;

  STAMP_RT(12);
  STAMP_AT(0);

  asm volatile("" ::"s"(idx), "s"(table), "s"(xw), "s"(ww), "s"(bw), "s"(W1), "s"(b1), "s"(W2), "s"(b2), "s"(out), "s"(ws));
  unsigned epoch, wv_[2] = {0, 0}, bwv = 0, b2v = 0, warm3 = 0;
#define LDH(dst, ptr) asm volatile("global_load_ushort %0, %1, off" : "=v"(dst) : "v"(ptr) : "memory")
  asm volatile("global_load_dword %0, %1, off sc1" : "=v"(epoch) : "v"(ws + mt * 128) : "memory");
  if (w == 0) {
    const int g = lane >> 3, g2 = g + 8 < ND_ ? g + 8 : ND_ - 1;
    LDH(wv_[0], ww + g); LDH(wv_[1], ww + g2); LDH(bwv, bw); LDH(b2v, b2);
    LDH(warm3, out + m0 + nt * 16);
  }
#undef LDH
  Ctx c;
  const __amdgpu_buffer_rsrc_t srdI = __builtin_amdgcn_make_buffer_rsrc((void*)idx, (short)0, B_ * F_ * 4, 0x00020000);
  {
    const unsigned vI = (unsigned)lane * 16u, p3 = w == 0 ? 12u : (unsigned)w + 8u;
    dma1<65536u, 0, 0>((unsigned)w * 1024u, vI, srdI, (unsigned)(m0 * F_ * 4 + w * 1024));
    dma1<65536u + 4096u, 4096u, 0>((unsigned)w * 1024u, vI, srdI, (unsigned)(m0 * F_ * 4 + w * 1024));
    dma1<65536u, 0, 0>(p3 * 1024u, vI, srdI, (unsigned)(m0 * F_ * 4) + p3 * 1024u);
    dma1<65536u, 0, 0>((w == 0 ? 8u : 12u) * 1024u, vI, srdI, (unsigned)(m0 * F_ * 4) + (w == 0 ? 8u : 12u) * 1024u);
  }
  unsigned warm = 0, warm2 = 0;
  {
    const int ln = mt * 64 + (tid >> 2);
    const unsigned short* p2 = table + (size_t)(ln < 2000 ? ln : 1999) * 64;
    const int t = tid < 208 ? tid : 207;
    const unsigned short* p = W1 + (size_t)(mt * 104 + (t >> 1)) * H_ + n0 + (t & 1) * 32;
    asm volatile("global_load_dword %0, %2, off\n\tglobal_load_dword %1, %3, off"
                 : "=&v"(warm2), "=&v"(warm) : "v"(p2), "v"(p) : "memory");
  }
  {
    const unsigned char* src = lane >= 16 && lane < 42 ? (const unsigned char*)(xw + (size_t)(m0 + nt * 16) * ND_) + (lane - 16) * 16
                               : (const unsigned char*)((lane >> 3) == 1 ? W2 : b1) + n0 * 2 + (lane & 7) * 16;
    asm volatile("s_mov_b32 m0, %0\n\ts_nop 0\n\tglobal_load_lds_dwordx4 %1, off" ::"s"(SMALL0), "v"(src) : "memory");
  }
  c.srdW = __builtin_amdgcn_make_buffer_rsrc((void*)W1, (short)0, WREC, 0x00020000);
  c.srdT = __builtin_amdgcn_make_buffer_rsrc((void*)table, (short)0, TREC, 0x00020000);
  c.ring = RING0 + w * 8192;
  c.wbase = (unsigned)(fh * 13 * 128 + ks * 16) * 1024u;
  c.tgrp = (unsigned)w * 4096u;
  c.vT[0] = (unsigned)(lane >> 2) * 256u + (unsigned)(((lane & 3) ^ ((lane >> 4) & 3)) * 16);
  c.vT[1] = c.vT[0] + 3072u; c.vT[2] = c.vT[0] + 6144u; c.vT[3] = c.vT[0] + 9216u;
  c.vW0 = (unsigned)(lane >> 3) * 1024u + (unsigned)(((lane & 7) ^ (((lane >> 4) & 1) << 2)) * 16) + (unsigned)n0 * 2u;
  c.vW1 = c.vW0 + 7168u;
  {
    const int qq = (lane & 15) >> 2, p = lane & 3, swz = (qq >> 1) & 1;
    const unsigned base = c.ring + (unsigned)((8 * (lane >> 5) + qq) * 128 + ((lane >> 4) & 1) * 32 + p * 8);
    c.tb0 = base + (unsigned)(swz * 64);
    c.tb1 = base + (unsigned)((swz ^ 1) * 64);
  }
  STAMP_AT(13);
  asm volatile("" ::: ACLOB);
  const unsigned tq0 = c.tgrp * 4u;
  dmaT<0>(c, c.tgrp, tq0); dmaT<1>(c, c.tgrp, tq0); Zero<0, 16>::run();
  dma1<0, 0, 0>(c.ring, c.vW0, c.srdW, c.wbase); dma1<0, 0, 1024>(c.ring, c.vW1, c.srdW, c.wbase); Zero<16, 16>::run();
  dmaT<2>(c, c.tgrp, tq0); dmaT<3>(c, c.tgrp, tq0); Zero<32, 16>::run();
  dmaT<4>(c, c.tgrp, tq0); dmaT<5>(c, c.tgrp, tq0); Zero<48, 16>::run();
  dmaT<6>(c, c.tgrp, tq0); dmaT<7>(c, c.tgrp, tq0); Zero<64, 16>::run();
  STAMP_AT(14);
  waitV<13>();
  bar();
  STAMP_AT(15);
  {
    const unsigned cs = (unsigned)(ks * 2 + (lane >> 5)) << 4;
#define ADDR_BLOCK(i)                                                                          \
    {                                                                                          \
      const int row = ((w + i) & 3) * 32 + (lane & 31);                                        \
      const int* ip = (const int*)(lds + 65536 + row * (F_ * 4) + fh * 52);                    \
      unsigned v[13];                                                                          \
      _Pragma("unroll") for (int j = 0; j < 13; ++j) v[j] = (unsigned)ip[j];                   \
      dmaT<8 + 2 * i>(c, c.tgrp, tq0); dmaT<9 + 2 * i>(c, c.tgrp, tq0);                  \
      Zero<80 + 12 * i, 12>::run();                                                            \
      _Pragma("unroll") for (int j = 0; j < 13; ++j)                                           \
        c.addr[j][i] = (v[j] << 6) | (((v[j] << 2) & 0x30u) ^ cs);                             \
    }
    ADDR_BLOCK(0) ADDR_BLOCK(1) ADDR_BLOCK(2) ADDR_BLOCK(3)
#undef ADDR_BLOCK
  }
  STAMP_AT(16);
  dma1<2048, 131072, 0>(c.ring, c.vW0, c.srdW, c.wbase); dma1<2048, 131072, 1024>(c.ring, c.vW1, c.srdW, c.wbase);
  dma1<4096, 2 * 131072, 0>(c.ring, c.vW0, c.srdW, c.wbase); dma1<4096, 2 * 131072, 1024>(c.ring, c.vW1, c.srdW, c.wbase);
  dma1<6144, 3 * 131072, 0>(c.ring, c.vW0, c.srdW, c.wbase); dma1<6144, 3 * 131072, 1024>(c.ring, c.vW1, c.srdW, c.wbase);
  STAMP_AT(1);
  waitL<0>();
  waitV<2 + 4>();
  asm volatile("" ::"v"(warm), "v"(warm2), "v"(warm3));
  bar();
  STAMP_AT(3);
  u32x2 b1v[8], w2v[8];
  unsigned xv[4];
  {
    const unsigned char* sp = lds + SMALL0 + (lane >> 5) * 8;
#pragma unroll
    for (int g = 0; g < 8; ++g) { b1v[g] = *(const u32x2*)(sp + g * 16); w2v[g] = *(const u32x2*)(sp + 128 + g * 16); }
    const int g = lane >> 3, g2 = g + 8 < ND_ ? g + 8 : ND_ - 1;
    const unsigned short* xp = (const unsigned short*)(lds + SMALL0 + 256) + (lane & 7) * 2 * ND_;
    xv[0] = xp[g]; xv[1] = xp[ND_ + g]; xv[2] = xp[g2]; xv[3] = xp[ND_ + g2];
  }
  readW<regW(2, 0), 0>(c.tb0);
  readW<regW(2, 1), 0>(c.tb1);
  readE<regE(2, 0)>(c.addr[0][0]); readE<regE(2, 1)>(c.addr[0][1]);
  readE<regE(2, 2)>(c.addr[0][2]); readE<regE(2, 3)>(c.addr[0][3]);

  unsigned slot = 0;
#pragma nounroll
  for (int q = 0; q < 4; ++q) {
    c.srdT = __builtin_amdgcn_make_buffer_rsrc((void*)table, (short)0, q == 3 ? 0 : TREC, 0x00020000);
    phase<false>(c, q, slot);
  }
  STAMP_AT(7);

#pragma unroll
  for (int g = 0; g < 8; ++g) asm volatile("" : "+v"(b1v[g]), "+v"(w2v[g]));
  asm volatile("s_nop 7\n\ts_waitcnt vmcnt(0) lgkmcnt(0)\n\ts_barrier"
               : "+v"(xv[0]), "+v"(xv[1]), "+v"(xv[2]), "+v"(xv[3]), "+v"(wv_[0]), "+v"(wv_[1]), "+v"(bwv), "+v"(b2v), "+v"(epoch)
               :: "memory");
  f32x4 h[8], w2f[8];
  const unsigned s1 = (unsigned)(RED0 + (((w + 1) & 3) * 3 + 0) * 8192 + lane * 16);
  const unsigned s2 = (unsigned)(RED0 + (((w + 2) & 3) * 3 + 1) * 8192 + lane * 16);
  const unsigned s3 = (unsigned)(RED0 + (((w + 3) & 3) * 3 + 2) * 8192 + lane * 16);
#define SEND(g)                                                                                                   \
  writeAcc<32 + g * 4, g * 1024>(s1);                                                                             \
  h[g] = readAcc4<g>();                                                                                           \
  h[g].x += __uint_as_float(b1v[g].x << 16); h[g].y += __uint_as_float(b1v[g].x & 0xffff0000u);                   \
  asm volatile("" : "+v"(h[g]));                                                                                  \
  writeAcc<64 + g * 4, g * 1024>(s2);                                                                             \
  h[g].z += __uint_as_float(b1v[g].y << 16); h[g].w += __uint_as_float(b1v[g].y & 0xffff0000u);                   \
  w2f[g].x = __uint_as_float(w2v[g].x << 16); w2f[g].y = __uint_as_float(w2v[g].x & 0xffff0000u);                 \
  asm volatile("" : "+v"(h[g]), "+v"(w2f[g].x), "+v"(w2f[g].y));                                                  \
  writeAcc<96 + g * 4, g * 1024>(s3);                                                                             \
  w2f[g].z = __uint_as_float(w2v[g].y << 16); w2f[g].w = __uint_as_float(w2v[g].y & 0xffff0000u);                 \
  asm volatile("" : "+v"(w2f[g].z), "+v"(w2f[g].w));
  SEND(0) SEND(1) SEND(2) SEND(3) SEND(4) SEND(5) SEND(6) SEND(7)
#undef SEND
  float wide = __uint_as_float(xv[0] << 16) * __uint_as_float(wv_[0] << 16), wide1 = __uint_as_float(xv[1] << 16) * __uint_as_float(wv_[0] << 16);
  if ((lane >> 3) + 8 < ND_) {
    wide += __uint_as_float(xv[2] << 16) * __uint_as_float(wv_[1] << 16);
    wide1 += __uint_as_float(xv[3] << 16) * __uint_as_float(wv_[1] << 16);
  }
  wide = sumX32(sumX16(sumX8(wide))); wide1 = sumX32(sumX16(sumX8(wide1)));
  asm volatile("s_waitcnt lgkmcnt(0)\n\ts_barrier" : "+v"(wide), "+v"(wide1) :: "memory");
  STAMP_AT(8);
  f32x4 r[24];
#pragma unroll
  for (int g = 0; g < 8; ++g) {
    const unsigned char* p = lds + RED0 + w * 3 * 8192 + g * 1024 + lane * 16;
    r[g * 3] = *(const f32x4*)p; r[g * 3 + 1] = *(const f32x4*)(p + 8192); r[g * 3 + 2] = *(const f32x4*)(p + 16384);
  }
  f32x4 pacc = {0.f, 0.f, 0.f, 0.f};
#pragma unroll
  for (int g = 0; g < 8; ++g) {
    if ((g & 1) == 0)
      asm volatile("" : "+v"(r[g * 3]), "+v"(r[g * 3 + 1]), "+v"(r[g * 3 + 2]), "+v"(r[g * 3 + 3]), "+v"(r[g * 3 + 4]), "+v"(r[g * 3 + 5]) :: "memory");
    const f32x4 s = (h[g] + r[g * 3]) + (r[g * 3 + 1] + r[g * 3 + 2]);
    pacc.x = fmaf(fmaxf(s.x, 0.f), w2f[g].x, pacc.x); pacc.y = fmaf(fmaxf(s.y, 0.f), w2f[g].y, pacc.y);
    pacc.z = fmaf(fmaxf(s.z, 0.f), w2f[g].z, pacc.z); pacc.w = fmaf(fmaxf(s.w, 0.f), w2f[g].w, pacc.w);
  }
  const float part = sumX32((pacc.x + pacc.y) + (pacc.z + pacc.w));

  const unsigned tag = epoch + 1u;
  gu64* gran = (gu64*)(ws + WS_GRAN);
  if (lane < 32)
    __hip_atomic_store(gran + (size_t)((mt * 8 + nt) * 128 + w * 32 + lane), ((unsigned long long)tag << 32) | __float_as_uint(part),
                       __ATOMIC_RELAXED, __HIP_MEMORY_SCOPE_AGENT);
  STAMP_AT(9);
  if (w == 0) {
    const unsigned char* gp = ws + WS_GRAN + (size_t)((mt * 8 + (lane >> 3)) * 128 + nt * 16 + (lane & 7) * 2) * 8;
    u32x4 gv;
#pragma unroll 1
    for (unsigned spins = 0; spins < (1u << 22); ++spins) {
      asm volatile("global_load_dwordx4 %0, %1, off sc1\n\ts_waitcnt vmcnt(0)" : "=v"(gv) : "v"(gp) : "memory");
      if (__all(gv.y == tag && gv.w == tag)) break;
    }
    float d0 = __uint_as_float(gv.x), d1 = __uint_as_float(gv.z);
    d0 = sumX32(sumX16(sumX8(d0))); d1 = sumX32(sumX16(sumX8(d1)));
    if (lane < 8) {
      const float bwf = __uint_as_float(bwv << 16), b2f = __uint_as_float(b2v << 16);
      const float z0 = rbf(rbf(rbf(wide) + bwf) + rbf(rbf(d0) + b2f)), z1 = rbf(rbf(rbf(wide1) + bwf) + rbf(rbf(d1) + b2f));
      __hip_bfloat162 o;
      o.x = __float2bfloat16(1.f / (1.f + __expf(-z0)));
      o.y = __float2bfloat16(1.f / (1.f + __expf(-z1)));
      *(__hip_bfloat162*)(out + m0 + nt * 16 + lane * 2) = o;
    }
    if (nt == 0 && lane == 0) __hip_atomic_store((gu32*)(ws + mt * 128), tag, __ATOMIC_RELAXED, __HIP_MEMORY_SCOPE_AGENT);
  }
}

extern "C" void kernel_launch(void* const* d_in, const int* in_sizes, int n_in, void* d_out, int out_size, void* d_ws,
                              size_t ws_size, hipStream_t stream) {
  (void)in_sizes; (void)n_in; (void)out_size;
  if (ws_size < (size_t)WS_GRAN + 32 * 8 * 128 * 8) return;
  (void)hipFuncSetAttribute((const void*)wide_deep_kernel, hipFuncAttributeMaxDynamicSharedMemorySize, LDS_BYTES);
  wide_deep_kernel<<<256, 256, LDS_BYTES, stream>>>(
      (const int*)d_in[0], (const unsigned short*)d_in[1], (const unsigned short*)d_in[2], (const unsigned short*)d_in[3],
      (const unsigned short*)d_in[4], (const unsigned short*)d_in[5], (const unsigned short*)d_in[6],
      (const unsigned short*)d_in[7], (const unsigned short*)d_in[8], (__hip_bfloat16*)d_out, (unsigned char*)d_ws);
}
```

```cpp
#include <hip/hip_runtime.h>
#include <hip/hip_bf16.h>
#include <stdint.h>

typedef unsigned u32x4 __attribute__((ext_vector_type(4)));
typedef unsigned u32x2 __attribute__((ext_vector_type(2)));
typedef float f32x4 __attribute__((ext_vector_type(4)));
typedef __attribute__((address_space(1))) unsigned long long gu64;
typedef __attribute__((address_space(1))) unsigned gu32;

constexpr int B_ = 4096, F_ = 26, D_ = 128, V_ = 1000, ND_ = 13, H_ = 512;
constexpr int LDS_BYTES = 163840;
constexpr int RING0 = 131072;
constexpr int SMALL0 = 64512;
constexpr int RED0 = 65536;
constexpr int WS_GRAN = 4096;

#define WREC (F_ * D_ * H_ * 2)
#define TREC (V_ * D_ * 2)
#define ACLOB "a0","a1","a2","a3","a4","a5","a6","a7","a8","a9","a10","a11","a12","a13","a14","a15", \
  "a16","a17","a18","a19","a20","a21","a22","a23","a24","a25","a26","a27","a28","a29","a30","a31", \
  "a32","a33","a34","a35","a36","a37","a38","a39","a40","a41","a42","a43","a44","a45","a46","a47", \
  "a48","a49","a50","a51","a52","a53","a54","a55","a56","a57","a58","a59","a60","a61","a62","a63", \
  "a64","a65","a66","a67","a68","a69","a70","a71","a72","a73","a74","a75","a76","a77","a78","a79", \
  "a80","a81","a82","a83","a84","a85","a86","a87","a88","a89","a90","a91","a92","a93","a94","a95", \
  "a96","a97","a98","a99","a100","a101","a102","a103","a104","a105","a106","a107","a108","a109","a110","a111", \
  "a112","a113","a114","a115","a116","a117","a118","a119","a120","a121","a122","a123","a124","a125","a126","a127", \
  "a128","a129","a130","a131","a132","a133","a134","a135","a136","a137","a138","a139","a140","a141","a142","a143", \
  "a144","a145","a146","a147","a148","a149","a150","a151","a152","a153","a154","a155","a156","a157","a158","a159", \
  "a160","a161","a162","a163","a164","a165","a166","a167","a168","a169","a170","a171","a172","a173","a174","a175", \
  "a176","a177","a178","a179","a180","a181","a182","a183","a184","a185","a186","a187","a188","a189","a190","a191", \
  "a192","a193","a194","a195","a196","a197","a198","a199"

struct Ctx {
  unsigned addr[13][4];
  unsigned tb0, tb1;
  unsigned vW0, vW1;
  unsigned vT[4];
  __amdgpu_buffer_rsrc_t srdW, srdT;
  unsigned ring;
  unsigned wbase;
  unsigned tgrp;
};

__host__ __device__ constexpr int setOf(int j) { return j == 0 ? 2 : (j & 1); }
__host__ __device__ constexpr int regE(int set, int i) { return 128 + set * 24 + i * 4; }
__host__ __device__ constexpr int regW(int set, int nb) { return 128 + set * 24 + 16 + nb * 4; }
__host__ __device__ constexpr int regC(int i, int nb) { return (i * 2 + nb) * 16; }

#define STAMP_AT(i) do {} while (0)
#define STAMP_RT(i) do {} while (0)
template <int N> __device__ __forceinline__ void waitV() { asm volatile("s_waitcnt vmcnt(%0)" ::"n"(N) : "memory"); }
template <int N> __device__ __forceinline__ void waitL() { asm volatile("s_waitcnt lgkmcnt(%0)" ::"n"(N) : "memory"); }
__device__ __forceinline__ void bar() { asm volatile("s_barrier" ::: "memory"); }

template <int R> __device__ __forceinline__ void readE(unsigned& a) {
  asm volatile("ds_read_b128 a[%c1:%c2], %0\n\tv_xor_b32 %0, 0x10000, %0" : "+v"(a) : "i"(R), "i"(R + 3) : "memory");
}
template <int R, int OFF> __device__ __forceinline__ void readW(unsigned a) {
  asm volatile("ds_read_b64_tr_b16 a[%c1:%c2], %0 offset:%c3\n\tds_read_b64_tr_b16 a[%c4:%c5], %0 offset:%c6"
               ::"v"(a), "i"(R), "i"(R + 1), "i"(OFF), "i"(R + 2), "i"(R + 3), "i"(OFF + 512) : "memory");
}
template <int C, int W, int E> __device__ __forceinline__ void mfma() {
  asm volatile("v_mfma_f32_32x32x16_bf16 a[%c0:%c1], a[%c2:%c3], a[%c4:%c5], a[%c0:%c1]"
               ::"i"(C), "i"(C + 15), "i"(W), "i"(W + 3), "i"(E), "i"(E + 3));
}
template <unsigned M, unsigned SO, int OFF>
__device__ __forceinline__ void dma1(unsigned m0a, unsigned voff, __amdgpu_buffer_rsrc_t srd, unsigned soa) {
  unsigned so;
  asm volatile("s_add_u32 m0, %1, %5\n\ts_add_u32 %0, %4, %6\n\tbuffer_load_dwordx4 %2, %3, %0 offen offset:%c7 lds"
               : "=&s"(so) : "s"(m0a), "v"(voff), "s"(srd), "s"(soa), "i"(M), "i"(SO), "i"(OFF) : "memory", "scc");
}

__host__ __device__ constexpr int nS(int j, bool last) { return (last || j < 0 || j > 10) ? 0 : (j == 10 ? 1 : 2 - (j & 1)); }
__host__ __device__ constexpr int firstS(int j) { int n = 0; for (int i = 0; i < j; ++i) n += nS(i, false); return n; }
__host__ __device__ constexpr int nB(int j, bool last) { return (!last || j <= 8) ? 2 : 0; }
__host__ __device__ constexpr int opsAll(int j, bool last) { return j < 0 ? 2 + nS(j + 13, false) : nB(j, last) + nS(j, last); }

template <int K> __device__ __forceinline__ void dmaT(const Ctx& c, unsigned m0a, unsigned soa) {
  constexpr int g = K >> 2, sub = K & 3;
  if constexpr (K == 15) m0a = c.tgrp == 3 * 4096u ? 130048u - 52224u : m0a;
  dma1<g * 16384u, g * 65536u, sub * 1024>(m0a, c.vT[sub], c.srdT, soa);
}

template <int J, bool LAST>
__device__ __forceinline__ void step(Ctx& c, unsigned wq, unsigned lb, unsigned tq, unsigned& slot) {
  constexpr int cur = setOf(J), JN = (J + 1) % 13, nxt = setOf(JN);
  constexpr bool hasNext = !(LAST && J == 12);
  constexpr int ns = nS(J, LAST), k0 = firstS(J);
  constexpr bool doW = nB(J, LAST) != 0;
  constexpr unsigned WO = (unsigned)((J + 4) % 13) * 131072u + ((J + 4) >= 13 ? 32768u : 0u);
  const unsigned slotN = (slot + 2048) & 8191;
  if constexpr (J == 12 && !LAST) { waitV<4>(); waitL<0>(); bar(); }
  else { if constexpr (hasNext) waitV<opsAll(J - 2, LAST) + opsAll(J - 1, LAST)>(); waitL<3>(); }
  const unsigned t0 = c.tb0 + slotN, t1 = c.tb1 + slotN, ms = c.ring + slot;
  mfma<regC(0, 0), regW(cur, 0), regE(cur, 0)>();
  if constexpr (hasNext) readW<regW(nxt, 0), 0>(t0);
  mfma<regC(0, 1), regW(cur, 1), regE(cur, 0)>();
  if constexpr (hasNext) readW<regW(nxt, 1), 0>(t1);
  if constexpr (hasNext) waitL<6>(); else waitL<2>();
  mfma<regC(1, 0), regW(cur, 0), regE(cur, 1)>();
  if constexpr (hasNext) readE<regE(nxt, 0)>(c.addr[JN][0]);
  if constexpr (ns >= 1) dmaT<k0>(c, lb, tq);
  mfma<regC(1, 1), regW(cur, 1), regE(cur, 1)>();
  if constexpr (doW) dma1<0, WO, 0>(ms, c.vW0, c.srdW, wq);
  if constexpr (hasNext) waitL<6>(); else waitL<1>();
  mfma<regC(2, 0), regW(cur, 0), regE(cur, 2)>();
  if constexpr (hasNext) readE<regE(nxt, 1)>(c.addr[JN][1]);
  if constexpr (ns >= 2) dmaT<k0 + 1>(c, lb, tq);
  mfma<regC(2, 1), regW(cur, 1), regE(cur, 2)>();
  if constexpr (doW) dma1<0, WO, 1024>(ms, c.vW1, c.srdW, wq);
  if constexpr (hasNext) waitL<6>(); else waitL<0>();
  mfma<regC(3, 0), regW(cur, 0), regE(cur, 3)>();
  if constexpr (hasNext) readE<regE(nxt, 2)>(c.addr[JN][2]);
  mfma<regC(3, 1), regW(cur, 1), regE(cur, 3)>();
  if constexpr (hasNext) readE<regE(nxt, 3)>(c.addr[JN][3]);
  slot = slotN;
}

template <bool LAST> __device__ __forceinline__ void phase(Ctx& c, int q, unsigned& slot) {
  const unsigned wq = c.wbase + (unsigned)q * 32768u, lb = (unsigned)(((q + 1) & 1) << 16) + c.tgrp,
                 tq = c.tgrp * 4u + (unsigned)(q + 1) * 64u;
  step<0, LAST>(c, wq, lb, tq, slot); step<1, LAST>(c, wq, lb, tq, slot); step<2, LAST>(c, wq, lb, tq, slot); step<3, LAST>(c, wq, lb, tq, slot);
  step<4, LAST>(c, wq, lb, tq, slot); step<5, LAST>(c, wq, lb, tq, slot); step<6, LAST>(c, wq, lb, tq, slot); step<7, LAST>(c, wq, lb, tq, slot);
  step<8, LAST>(c, wq, lb, tq, slot); step<9, LAST>(c, wq, lb, tq, slot); step<10, LAST>(c, wq, lb, tq, slot); step<11, LAST>(c, wq, lb, tq, slot);
  step<12, LAST>(c, wq, lb, tq, slot);
}

template <int R> __device__ __forceinline__ void zero1() { asm volatile("v_accvgpr_write_b32 a[%c0], 0" ::"i"(R)); }
template <int R, int N> struct Zero { static __device__ __forceinline__ void run() { zero1<R>(); Zero<R + 1, N - 1>::run(); } };
template <int R> struct Zero<R, 0> { static __device__ __forceinline__ void run() {} };

template <int R, int OFF> __device__ __forceinline__ void writeAcc(unsigned a) {
  asm volatile("ds_write_b128 %0, a[%c1:%c2] offset:%c3" ::"v"(a), "i"(R), "i"(R + 3), "i"(OFF) : "memory");
}
template <int R> __device__ __forceinline__ float readAcc() {
  float v; asm volatile("v_accvgpr_read_b32 %0, a[%c1]" : "=v"(v) : "i"(R)); return v;
}
template <int G> __device__ __forceinline__ f32x4 readAcc4() {
  f32x4 v; v.x = readAcc<G * 4>(); v.y = readAcc<G * 4 + 1>(); v.z = readAcc<G * 4 + 2>(); v.w = readAcc<G * 4 + 3>(); return v;
}

__device__ __forceinline__ float sumX8(float x) {
  return x + __int_as_float(__builtin_amdgcn_update_dpp(0, __float_as_int(x), 0x128, 0xf, 0xf, true));
}
__device__ __forceinline__ float sumX16(float x) {
  const auto r = __builtin_amdgcn_permlane16_swap(__float_as_uint(x), __float_as_uint(x), false, false);
  return __uint_as_float(r[0]) + __uint_as_float(r[1]);
}
__device__ __forceinline__ float sumX32(float x) {
  const auto r = __builtin_amdgcn_permlane32_swap(__float_as_uint(x), __float_as_uint(x), false, false);
  return __uint_as_float(r[0]) + __uint_as_float(r[1]);
}
__device__ __forceinline__ float rbf(float x) { return __bfloat162float(__float2bfloat16(x)); }

__global__ __launch_bounds__(256, 1) void wide_deep_kernel(
    const int* __restrict__ idx, const unsigned short* __restrict__ table, const unsigned short* __restrict__ xw,
    const unsigned short* __restrict__ ww, const unsigned short* __restrict__ bw, const unsigned short* __restrict__ W1,
    const unsigned short* __restrict__ b1, const unsigned short* __restrict__ W2, const unsigned short* __restrict__ b2,
    __hip_bfloat16* __restrict__ out, unsigned char* ws) {
  extern __shared__ __attribute__((aligned(16))) unsigned char lds[];
  unsigned warm4 = 0;
  {
    unsigned long long pc;
    asm volatile("s_getpc_b64 %1\n.Lwd_pc:\n\tv_lshlrev_b32 %0, 7, %2\n\tv_min_u32 %0, .Lwd_end-.Lwd_pc, %0\n\t"
                 "global_load_dword %0, %0, %1" : "=&v"(warm4), "=&s"(pc) : "v"(threadIdx.x) : "memory");
    __builtin_amdgcn_sched_barrier(0);
  }
  const int tid = threadIdx.x, lane = tid & 63;
  const int w = __builtin_amdgcn_readfirstlane(tid >> 6);
  const int nt = blockIdx.x & 7, mt = blockIdx.x >> 3;
  const int n0 = nt * 64, m0 = mt * 128;
  const int fh = w >> 1, ks = w & 1;

  STAMP_RT(12);
  STAMP_AT(0);

  asm volatile("" ::"s"(idx), "s"(table), "s"(xw), "s"(ww), "s"(bw), "s"(W1), "s"(b1), "s"(W2), "s"(b2), "s"(out), "s"(ws));
  unsigned epoch, wv_[2] = {0, 0}, bwv = 0, b2v = 0, warm3 = 0;
#define LDH(dst, ptr) asm volatile("global_load_ushort %0, %1, off" : "=v"(dst) : "v"(ptr) : "memory")
  asm volatile("global_load_dword %0, %1, off sc1" : "=v"(epoch) : "v"(ws + mt * 128) : "memory");
  if (w == 0) {
    const int g = lane >> 3, g2 = g + 8 < ND_ ? g + 8 : ND_ - 1;
    LDH(wv_[0], ww + g); LDH(wv_[1], ww + g2); LDH(bwv, bw); LDH(b2v, b2);
    LDH(warm3, out + m0 + nt * 16);
  }
#undef LDH
  Ctx c;
  const __amdgpu_buffer_rsrc_t srdI = __builtin_amdgcn_make_buffer_rsrc((void*)idx, (short)0, B_ * F_ * 4, 0x00020000);
  {
    const unsigned vI = (unsigned)lane * 16u, p3 = w == 0 ? 12u : (unsigned)w + 8u;
    dma1<65536u, 0, 0>((unsigned)w * 1024u, vI, srdI, (unsigned)(m0 * F_ * 4 + w * 1024));
    dma1<65536u + 4096u, 4096u, 0>((unsigned)w * 1024u, vI, srdI, (unsigned)(m0 * F_ * 4 + w * 1024));
    dma1<65536u, 0, 0>(p3 * 1024u, vI, srdI, (unsigned)(m0 * F_ * 4) + p3 * 1024u);
    dma1<65536u, 0, 0>((w == 0 ? 8u : 12u) * 1024u, vI, srdI, (unsigned)(m0 * F_ * 4) + (w == 0 ? 8u : 12u) * 1024u);
  }
  unsigned warm = 0, warm2 = 0;
  {
    const int ln = mt * 64 + (tid >> 2);
    const unsigned short* p2 = table + (size_t)(ln < 2000 ? ln : 1999) * 64;
    const int t = tid < 208 ? tid : 207;
    const unsigned short* p = W1 + (size_t)(mt * 104 + (t >> 1)) * H_ + n0 + (t & 1) * 32;
    asm volatile("global_load_dword %0, %2, off\n\tglobal_load_dword %1, %3, off"
                 : "=&v"(warm2), "=&v"(warm) : "v"(p2), "v"(p) : "memory");
  }
  {
    const unsigned char* src = lane >= 16 && lane < 42 ? (const unsigned char*)(xw + (size_t)(m0 + nt * 16) * ND_) + (lane - 16) * 16
                               : (const unsigned char*)((lane >> 3) == 1 ? W2 : b1) + n0 * 2 + (lane & 7) * 16;
    asm volatile("s_mov_b32 m0, %0\n\ts_nop 0\n\tglobal_load_lds_dwordx4 %1, off" ::"s"(SMALL0), "v"(src) : "memory");
  }
  c.srdW = __builtin_amdgcn_make_buffer_rsrc((void*)W1, (short)0, WREC, 0x00020000);
  c.srdT = __builtin_amdgcn_make_buffer_rsrc((void*)table, (short)0, TREC, 0x00020000);
  c.ring = RING0 + w * 8192;
  c.wbase = (unsigned)(fh * 13 * 128 + ks * 16) * 1024u;
  c.tgrp = (unsigned)w * 4096u;
  c.vT[0] = (unsigned)(lane >> 2) * 256u + (unsigned)(((lane & 3) ^ ((lane >> 4) & 3)) * 16);
  c.vT[1] = c.vT[0] + 3072u; c.vT[2] = c.vT[0] + 6144u; c.vT[3] = c.vT[0] + 9216u;
  c.vW0 = (unsigned)(lane >> 3) * 1024u + (unsigned)(((lane & 7) ^ (((lane >> 4) & 1) << 2)) * 16) + (unsigned)n0 * 2u;
  c.vW1 = c.vW0 + 7168u;
  {
    const int qq = (lane & 15) >> 2, p = lane & 3, swz = (qq >> 1) & 1;
    const unsigned base = c.ring + (unsigned)((8 * (lane >> 5) + qq) * 128 + ((lane >> 4) & 1) * 32 + p * 8);
    c.tb0 = base + (unsigned)(swz * 64);
    c.tb1 = base + (unsigned)((swz ^ 1) * 64);
  }
  STAMP_AT(13);
  asm volatile("" ::: ACLOB);
  const unsigned tq0 = c.tgrp * 4u;
  dmaT<0>(c, c.tgrp, tq0); dmaT<1>(c, c.tgrp, tq0); Zero<0, 16>::run();
  dma1<0, 0, 0>(c.ring, c.vW0, c.srdW, c.wbase); dma1<0, 0, 1024>(c.ring, c.vW1, c.srdW, c.wbase); Zero<16, 16>::run();
  dmaT<2>(c, c.tgrp, tq0); dmaT<3>(c, c.tgrp, tq0); Zero<32, 16>::run();
  dmaT<4>(c, c.tgrp, tq0); dmaT<5>(c, c.tgrp, tq0); Zero<48, 16>::run();
  dmaT<6>(c, c.tgrp, tq0); dmaT<7>(c, c.tgrp, tq0); Zero<64, 16>::run();
  dmaT<8>(c, c.tgrp, tq0); dmaT<9>(c, c.tgrp, tq0); Zero<80, 12>::run();
  dmaT<10>(c, c.tgrp, tq0); dmaT<11>(c, c.tgrp, tq0); Zero<92, 12>::run();
  dmaT<12>(c, c.tgrp, tq0); dmaT<13>(c, c.tgrp, tq0); Zero<104, 12>::run();
  dmaT<14>(c, c.tgrp, tq0); dmaT<15>(c, c.tgrp, tq0); Zero<116, 12>::run();
  STAMP_AT(14);
  waitV<2 + 1 + 18>();
  bar();
  STAMP_AT(15);
  {
    const unsigned cs = (unsigned)(ks * 2 + (lane >> 5)) << 4;
#define ADDR_BLOCK(i)                                                                          \
    {                                                                                          \
      const int row = ((w + i) & 3) * 32 + (lane & 31);                                        \
      const int* ip = (const int*)(lds + 65536 + row * (F_ * 4) + fh * 52);                    \
      unsigned v[13];                                                                          \
      _Pragma("unroll") for (int j = 0; j < 13; ++j) v[j] = (unsigned)ip[j];                   \
      if constexpr (i < 3) {                                                                   \
        dma1<2048 * (i + 1), 131072 * (i + 1), 0>(c.ring, c.vW0, c.srdW, c.wbase);             \
        dma1<2048 * (i + 1), 131072 * (i + 1), 1024>(c.ring, c.vW1, c.srdW, c.wbase);          \
      }                                                                                        \
      _Pragma("unroll") for (int j = 0; j < 13; ++j)                                           \
        c.addr[j][i] = (v[j] << 6) | (((v[j] << 2) & 0x30u) ^ cs);                             \
    }
    ADDR_BLOCK(0) ADDR_BLOCK(1) ADDR_BLOCK(2) ADDR_BLOCK(3)
#undef ADDR_BLOCK
  }
  STAMP_AT(16);
  STAMP_AT(1);
  waitL<0>();
  waitV<2 + 4>();
  asm volatile("" ::"v"(warm), "v"(warm2), "v"(warm3), "v"(warm4));
  bar();
  STAMP_AT(3);
  u32x2 b1v[8], w2v[8];
  unsigned xv[4];
  {
    const unsigned char* sp = lds + SMALL0 + (lane >> 5) * 8;
#pragma unroll
    for (int g = 0; g < 8; ++g) { b1v[g] = *(const u32x2*)(sp + g * 16); w2v[g] = *(const u32x2*)(sp + 128 + g * 16); }
    const int g = lane >> 3, g2 = g + 8 < ND_ ? g + 8 : ND_ - 1;
    const unsigned short* xp = (const unsigned short*)(lds + SMALL0 + 256) + (lane & 7) * 2 * ND_;
    xv[0] = xp[g]; xv[1] = xp[ND_ + g]; xv[2] = xp[g2]; xv[3] = xp[ND_ + g2];
  }
  readW<regW(2, 0), 0>(c.tb0);
  readW<regW(2, 1), 0>(c.tb1);
  readE<regE(2, 0)>(c.addr[0][0]); readE<regE(2, 1)>(c.addr[0][1]);
  readE<regE(2, 2)>(c.addr[0][2]); readE<regE(2, 3)>(c.addr[0][3]);

  unsigned slot = 0;
#pragma nounroll
  for (int q = 0; q < 4; ++q) {
    c.srdT = __builtin_amdgcn_make_buffer_rsrc((void*)table, (short)0, q == 3 ? 0 : TREC, 0x00020000);
    phase<false>(c, q, slot);
  }
  STAMP_AT(7);

#pragma unroll
  for (int g = 0; g < 8; ++g) asm volatile("" : "+v"(b1v[g]), "+v"(w2v[g]));
  asm volatile("s_nop 7\n\ts_waitcnt vmcnt(0) lgkmcnt(0)\n\ts_barrier"
               : "+v"(xv[0]), "+v"(xv[1]), "+v"(xv[2]), "+v"(xv[3]), "+v"(wv_[0]), "+v"(wv_[1]), "+v"(bwv), "+v"(b2v), "+v"(epoch)
               :: "memory");
  f32x4 h[8], w2f[8];
  const unsigned s1 = (unsigned)(RED0 + (((w + 1) & 3) * 3 + 0) * 8192 + lane * 16);
  const unsigned s2 = (unsigned)(RED0 + (((w + 2) & 3) * 3 + 1) * 8192 + lane * 16);
  const unsigned s3 = (unsigned)(RED0 + (((w + 3) & 3) * 3 + 2) * 8192 + lane * 16);
#define SEND(g)                                                                                                   \
  writeAcc<32 + g * 4, g * 1024>(s1);                                                                             \
  h[g] = readAcc4<g>();                                                                                           \
  h[g].x += __uint_as_float(b1v[g].x << 16); h[g].y += __uint_as_float(b1v[g].x & 0xffff0000u);                   \
  asm volatile("" : "+v"(h[g]));                                                                                  \
  writeAcc<64 + g * 4, g * 1024>(s2);                                                                             \
  h[g].z += __uint_as_float(b1v[g].y << 16); h[g].w += __uint_as_float(b1v[g].y & 0xffff0000u);                   \
  w2f[g].x = __uint_as_float(w2v[g].x << 16); w2f[g].y = __uint_as_float(w2v[g].x & 0xffff0000u);                 \
  asm volatile("" : "+v"(h[g]), "+v"(w2f[g].x), "+v"(w2f[g].y));                                                  \
  writeAcc<96 + g * 4, g * 1024>(s3);                                                                             \
  w2f[g].z = __uint_as_float(w2v[g].y << 16); w2f[g].w = __uint_as_float(w2v[g].y & 0xffff0000u);                 \
  asm volatile("" : "+v"(w2f[g].z), "+v"(w2f[g].w));
  SEND(0) SEND(1) SEND(2) SEND(3) SEND(4) SEND(5) SEND(6) SEND(7)
#undef SEND
  float wide = __uint_as_float(xv[0] << 16) * __uint_as_float(wv_[0] << 16), wide1 = __uint_as_float(xv[1] << 16) * __uint_as_float(wv_[0] << 16);
  if ((lane >> 3) + 8 < ND_) {
    wide += __uint_as_float(xv[2] << 16) * __uint_as_float(wv_[1] << 16);
    wide1 += __uint_as_float(xv[3] << 16) * __uint_as_float(wv_[1] << 16);
  }
  wide = sumX32(sumX16(sumX8(wide))); wide1 = sumX32(sumX16(sumX8(wide1)));
  asm volatile("s_waitcnt lgkmcnt(0)\n\ts_barrier" : "+v"(wide), "+v"(wide1) :: "memory");
  STAMP_AT(8);
  f32x4 r[24];
#pragma unroll
  for (int g = 0; g < 8; ++g) {
    const unsigned char* p = lds + RED0 + w * 3 * 8192 + g * 1024 + lane * 16;
    r[g * 3] = *(const f32x4*)p; r[g * 3 + 1] = *(const f32x4*)(p + 8192); r[g * 3 + 2] = *(const f32x4*)(p + 16384);
  }
  f32x4 pacc = {0.f, 0.f, 0.f, 0.f};
#pragma unroll
  for (int g = 0; g < 8; ++g) {
    if ((g & 1) == 0)
      asm volatile("" : "+v"(r[g * 3]), "+v"(r[g * 3 + 1]), "+v"(r[g * 3 + 2]), "+v"(r[g * 3 + 3]), "+v"(r[g * 3 + 4]), "+v"(r[g * 3 + 5]) :: "memory");
    const f32x4 s = (h[g] + r[g * 3]) + (r[g * 3 + 1] + r[g * 3 + 2]);
    pacc.x = fmaf(fmaxf(s.x, 0.f), w2f[g].x, pacc.x); pacc.y = fmaf(fmaxf(s.y, 0.f), w2f[g].y, pacc.y);
    pacc.z = fmaf(fmaxf(s.z, 0.f), w2f[g].z, pacc.z); pacc.w = fmaf(fmaxf(s.w, 0.f), w2f[g].w, pacc.w);
  }
  const float part = sumX32((pacc.x + pacc.y) + (pacc.z + pacc.w));

  const unsigned tag = epoch + 1u;
  gu64* gran = (gu64*)(ws + WS_GRAN);
  if (lane < 32)
    __hip_atomic_store(gran + (size_t)((mt * 8 + nt) * 128 + w * 32 + lane), ((unsigned long long)tag << 32) | __float_as_uint(part),
                       __ATOMIC_RELAXED, __HIP_MEMORY_SCOPE_AGENT);
  STAMP_AT(9);
  if (w == 0) {
    const unsigned char* gp = ws + WS_GRAN + (size_t)((mt * 8 + (lane >> 3)) * 128 + nt * 16 + (lane & 7) * 2) * 8;
    u32x4 gv;
#pragma unroll 1
    for (unsigned spins = 0; spins < (1u << 22); ++spins) {
      asm volatile("global_load_dwordx4 %0, %1, off sc1\n\ts_waitcnt vmcnt(0)" : "=v"(gv) : "v"(gp) : "memory");
      if (__all(gv.y == tag && gv.w == tag)) break;
    }
    float d0 = __uint_as_float(gv.x), d1 = __uint_as_float(gv.z);
    d0 = sumX32(sumX16(sumX8(d0))); d1 = sumX32(sumX16(sumX8(d1)));
    if (lane < 8) {
      const float bwf = __uint_as_float(bwv << 16), b2f = __uint_as_float(b2v << 16);
      const float z0 = rbf(rbf(rbf(wide) + bwf) + rbf(rbf(d0) + b2f)), z1 = rbf(rbf(rbf(wide1) + bwf) + rbf(rbf(d1) + b2f));
      __hip_bfloat162 o;
      o.x = __float2bfloat16(1.f / (1.f + __expf(-z0)));
      o.y = __float2bfloat16(1.f / (1.f + __expf(-z1)));
      *(__hip_bfloat162*)(out + m0 + nt * 16 + lane * 2) = o;
    }
    if (nt == 0 && lane == 0) __hip_atomic_store((gu32*)(ws + mt * 128), tag, __ATOMIC_RELAXED, __HIP_MEMORY_SCOPE_AGENT);
  }
  asm volatile(".Lwd_end:\n\ts_nop 0");
}

extern "C" void kernel_launch(void* const* d_in, const int* in_sizes, int n_in, void* d_out, int out_size, void* d_ws,
                              size_t ws_size, hipStream_t stream) {
  (void)in_sizes; (void)n_in; (void)out_size;
  if (ws_size < (size_t)WS_GRAN + 32 * 8 * 128 * 8) return;
  (void)hipFuncSetAttribute((const void*)wide_deep_kernel, hipFuncAttributeMaxDynamicSharedMemorySize, LDS_BYTES);
  wide_deep_kernel<<<256, 256, LDS_BYTES, stream>>>(
      (const int*)d_in[0], (const unsigned short*)d_in[1], (const unsigned short*)d_in[2], (const unsigned short*)d_in[3],
      (const unsigned short*)d_in[4], (const unsigned short*)d_in[5], (const unsigned short*)d_in[6],
      (const unsigned short*)d_in[7], (const unsigned short*)d_in[8], (__hip_bfloat16*)d_out, (unsigned char*)d_ws);
}
```

```cpp
#include <hip/hip_runtime.h>
#include <hip/hip_bf16.h>
#include <stdint.h>

typedef unsigned u32x4 __attribute__((ext_vector_type(4)));
typedef unsigned u32x2 __attribute__((ext_vector_type(2)));
typedef float f32x4 __attribute__((ext_vector_type(4)));
typedef __attribute__((address_space(1))) unsigned long long gu64;
typedef __attribute__((address_space(1))) unsigned gu32;

constexpr int B_ = 4096, F_ = 26, D_ = 128, V_ = 1000, ND_ = 13, H_ = 512;
constexpr int LDS_BYTES = 163840;
constexpr int RING0 = 131072;
constexpr int SMALL0 = 64512;
constexpr int RED0 = 65536;
constexpr int WS_GRAN = 4096;

#define WREC (F_ * D_ * H_ * 2)
#define TREC (V_ * D_ * 2)
#define ACLOB "a0","a1","a2","a3","a4","a5","a6","a7","a8","a9","a10","a11","a12","a13","a14","a15", \
  "a16","a17","a18","a19","a20","a21","a22","a23","a24","a25","a26","a27","a28","a29","a30","a31", \
  "a32","a33","a34","a35","a36","a37","a38","a39","a40","a41","a42","a43","a44","a45","a46","a47", \
  "a48","a49","a50","a51","a52","a53","a54","a55","a56","a57","a58","a59","a60","a61","a62","a63", \
  "a64","a65","a66","a67","a68","a69","a70","a71","a72","a73","a74","a75","a76","a77","a78","a79", \
  "a80","a81","a82","a83","a84","a85","a86","a87","a88","a89","a90","a91","a92","a93","a94","a95", \
  "a96","a97","a98","a99","a100","a101","a102","a103","a104","a105","a106","a107","a108","a109","a110","a111", \
  "a112","a113","a114","a115","a116","a117","a118","a119","a120","a121","a122","a123","a124","a125","a126","a127", \
  "a128","a129","a130","a131","a132","a133","a134","a135","a136","a137","a138","a139","a140","a141","a142","a143", \
  "a144","a145","a146","a147","a148","a149","a150","a151","a152","a153","a154","a155","a156","a157","a158","a159", \
  "a160","a161","a162","a163","a164","a165","a166","a167","a168","a169","a170","a171","a172","a173","a174","a175", \
  "a176","a177","a178","a179","a180","a181","a182","a183","a184","a185","a186","a187","a188","a189","a190","a191", \
  "a192","a193","a194","a195","a196","a197","a198","a199"

struct Ctx {
  unsigned addr[13][4];
  unsigned tb0, tb1;
  unsigned vW0, vW1;
  unsigned vT[4];
  __amdgpu_buffer_rsrc_t srdW, srdT;
  unsigned ring;
  unsigned wbase;
  unsigned tgrp;
};

__host__ __device__ constexpr int setOf(int j) { return j == 0 ? 2 : (j & 1); }
__host__ __device__ constexpr int regE(int set, int i) { return 128 + set * 24 + i * 4; }
__host__ __device__ constexpr int regW(int set, int nb) { return 128 + set * 24 + 16 + nb * 4; }
__host__ __device__ constexpr int regC(int i, int nb) { return (i * 2 + nb) * 16; }

#define STAMP_AT(i) do {} while (0)
#define STAMP_RT(i) do {} while (0)
template <int N> __device__ __forceinline__ void waitV() { asm volatile("s_waitcnt vmcnt(%0)" ::"n"(N) : "memory"); }
template <int N> __device__ __forceinline__ void waitL() { asm volatile("s_waitcnt lgkmcnt(%0)" ::"n"(N) : "memory"); }
__device__ __forceinline__ void bar() { asm volatile("s_barrier" ::: "memory"); }

template <int R> __device__ __forceinline__ void readE(unsigned& a) {
  asm volatile("ds_read_b128 a[%c1:%c2], %0\n\tv_xor_b32 %0, 0x10000, %0" : "+v"(a) : "i"(R), "i"(R + 3) : "memory");
}
template <int R, int OFF> __device__ __forceinline__ void readW(unsigned a) {
  asm volatile("ds_read_b64_tr_b16 a[%c1:%c2], %0 offset:%c3\n\tds_read_b64_tr_b16 a[%c4:%c5], %0 offset:%c6"
               ::"v"(a), "i"(R), "i"(R + 1), "i"(OFF), "i"(R + 2), "i"(R + 3), "i"(OFF + 512) : "memory");
}
template <int C, int W, int E> __device__ __forceinline__ void mfma() {
  asm volatile("v_mfma_f32_32x32x16_bf16 a[%c0:%c1], a[%c2:%c3], a[%c4:%c5], a[%c0:%c1]"
               ::"i"(C), "i"(C + 15), "i"(W), "i"(W + 3), "i"(E), "i"(E + 3));
}
template <unsigned M, unsigned SO, int OFF>
__device__ __forceinline__ void dma1(unsigned m0a, unsigned voff, __amdgpu_buffer_rsrc_t srd, unsigned soa) {
  unsigned so;
  asm volatile("s_add_u32 m0, %1, %5\n\ts_add_u32 %0, %4, %6\n\tbuffer_load_dwordx4 %2, %3, %0 offen offset:%c7 lds"
               : "=&s"(so) : "s"(m0a), "v"(voff), "s"(srd), "s"(soa), "i"(M), "i"(SO), "i"(OFF) : "memory", "scc");
}

__host__ __device__ constexpr int nS(int j, bool last) { return (last || j < 0 || j > 10) ? 0 : (j == 10 ? 1 : 2 - (j & 1)); }
__host__ __device__ constexpr int firstS(int j) { int n = 0; for (int i = 0; i < j; ++i) n += nS(i, false); return n; }
__host__ __device__ constexpr int nB(int j, bool last) { return (!last || j <= 8) ? 2 : 0; }
__host__ __device__ constexpr int opsAll(int j, bool last) { return j < 0 ? 2 + nS(j + 13, false) : nB(j, last) + nS(j, last); }

template <int K> __device__ __forceinline__ void dmaT(const Ctx& c, unsigned m0a, unsigned soa) {
  constexpr int g = K >> 2, sub = K & 3;
  if constexpr (K == 15) m0a = c.tgrp == 3 * 4096u ? 130048u - 52224u : m0a;
  dma1<g * 16384u, g * 65536u, sub * 1024>(m0a, c.vT[sub], c.srdT, soa);
}

template <int J, bool LAST>
__device__ __forceinline__ void step(Ctx& c, unsigned wq, unsigned lb, unsigned tq, unsigned& slot) {
  constexpr int cur = setOf(J), JN = (J + 1) % 13, nxt = setOf(JN);
  constexpr bool hasNext = !(LAST && J == 12);
  constexpr int ns = nS(J, LAST), k0 = firstS(J);
  constexpr bool doW = nB(J, LAST) != 0;
  constexpr unsigned WO = (unsigned)((J + 4) % 13) * 131072u + ((J + 4) >= 13 ? 32768u : 0u);
  const unsigned slotN = (slot + 2048) & 8191;
  if constexpr (J == 12 && !LAST) { waitV<4>(); waitL<0>(); bar(); }
  else { if constexpr (hasNext) waitV<opsAll(J - 2, LAST) + opsAll(J - 1, LAST)>(); waitL<3>(); }
  const unsigned t0 = c.tb0 + slotN, t1 = c.tb1 + slotN, ms = c.ring + slot;
  mfma<regC(0, 0), regW(cur, 0), regE(cur, 0)>();
  if constexpr (hasNext) readW<regW(nxt, 0), 0>(t0);
  mfma<regC(0, 1), regW(cur, 1), regE(cur, 0)>();
  if constexpr (hasNext) readW<regW(nxt, 1), 0>(t1);
  if constexpr (hasNext) waitL<6>(); else waitL<2>();
  mfma<regC(1, 0), regW(cur, 0), regE(cur, 1)>();
  if constexpr (hasNext) readE<regE(nxt, 0)>(c.addr[JN][0]);
  if constexpr (ns >= 1) dmaT<k0>(c, lb, tq);
  mfma<regC(1, 1), regW(cur, 1), regE(cur, 1)>();
  if constexpr (doW) dma1<0, WO, 0>(ms, c.vW0, c.srdW, wq);
  if constexpr (hasNext) waitL<6>(); else waitL<1>();
  mfma<regC(2, 0), regW(cur, 0), regE(cur, 2)>();
  if constexpr (hasNext) readE<regE(nxt, 1)>(c.addr[JN][1]);
  if constexpr (ns >= 2) dmaT<k0 + 1>(c, lb, tq);
  mfma<regC(2, 1), regW(cur, 1), regE(cur, 2)>();
  if constexpr (doW) dma1<0, WO, 1024>(ms, c.vW1, c.srdW, wq);
  if constexpr (hasNext) waitL<6>(); else waitL<0>();
  mfma<regC(3, 0), regW(cur, 0), regE(cur, 3)>();
  if constexpr (hasNext) readE<regE(nxt, 2)>(c.addr[JN][2]);
  mfma<regC(3, 1), regW(cur, 1), regE(cur, 3)>();
  if constexpr (hasNext) readE<regE(nxt, 3)>(c.addr[JN][3]);
  slot = slotN;
}

template <bool LAST> __device__ __forceinline__ void phase(Ctx& c, int q, unsigned& slot) {
  const unsigned wq = c.wbase + (unsigned)q * 32768u, lb = (unsigned)(((q + 1) & 1) << 16) + c.tgrp,
                 tq = c.tgrp * 4u + (unsigned)(q + 1) * 64u;
  step<0, LAST>(c, wq, lb, tq, slot); step<1, LAST>(c, wq, lb, tq, slot); step<2, LAST>(c, wq, lb, tq, slot); step<3, LAST>(c, wq, lb, tq, slot);
  step<4, LAST>(c, wq, lb, tq, slot); step<5, LAST>(c, wq, lb, tq, slot); step<6, LAST>(c, wq, lb, tq, slot); step<7, LAST>(c, wq, lb, tq, slot);
  step<8, LAST>(c, wq, lb, tq, slot); step<9, LAST>(c, wq, lb, tq, slot); step<10, LAST>(c, wq, lb, tq, slot); step<11, LAST>(c, wq, lb, tq, slot);
  step<12, LAST>(c, wq, lb, tq, slot);
}

template <int R> __device__ __forceinline__ void zero1() { asm volatile("v_accvgpr_write_b32 a[%c0], 0" ::"i"(R)); }
template <int R, int N> struct Zero { static __device__ __forceinline__ void run() { zero1<R>(); Zero<R + 1, N - 1>::run(); } };
template <int R> struct Zero<R, 0> { static __device__ __forceinline__ void run() {} };

template <int R, int OFF> __device__ __forceinline__ void writeAcc(unsigned a) {
  asm volatile("ds_write_b128 %0, a[%c1:%c2] offset:%c3" ::"v"(a), "i"(R), "i"(R + 3), "i"(OFF) : "memory");
}
template <int R> __device__ __forceinline__ float readAcc() {
  float v; asm volatile("v_accvgpr_read_b32 %0, a[%c1]" : "=v"(v) : "i"(R)); return v;
}
template <int G> __device__ __forceinline__ f32x4 readAcc4() {
  f32x4 v; v.x = readAcc<G * 4>(); v.y = readAcc<G * 4 + 1>(); v.z = readAcc<G * 4 + 2>(); v.w = readAcc<G * 4 + 3>(); return v;
}

__device__ __forceinline__ float sumX8(float x) {
  return x + __int_as_float(__builtin_amdgcn_update_dpp(0, __float_as_int(x), 0x128, 0xf, 0xf, true));
}
__device__ __forceinline__ float sumX16(float x) {
  const auto r = __builtin_amdgcn_permlane16_swap(__float_as_uint(x), __float_as_uint(x), false, false);
  return __uint_as_float(r[0]) + __uint_as_float(r[1]);
}
__device__ __forceinline__ float sumX32(float x) {
  const auto r = __builtin_amdgcn_permlane32_swap(__float_as_uint(x), __float_as_uint(x), false, false);
  return __uint_as_float(r[0]) + __uint_as_float(r[1]);
}
__device__ __forceinline__ float rbf(float x) { return __bfloat162float(__float2bfloat16(x)); }

__global__ __launch_bounds__(256, 1) void wide_deep_kernel(
    const int* __restrict__ idx, const unsigned short* __restrict__ table, const unsigned short* __restrict__ xw,
    const unsigned short* __restrict__ ww, const unsigned short* __restrict__ bw, const unsigned short* __restrict__ W1,
    const unsigned short* __restrict__ b1, const unsigned short* __restrict__ W2, const unsigned short* __restrict__ b2,
    __hip_bfloat16* __restrict__ out, unsigned char* ws) {
  extern __shared__ __attribute__((aligned(16))) unsigned char lds[];
  const int tid = threadIdx.x, lane = tid & 63;
  const int w = __builtin_amdgcn_readfirstlane(tid >> 6);
  const int nt = blockIdx.x & 7, mt = blockIdx.x >> 3;
  const int n0 = nt * 64, m0 = mt * 128;
  const int fh = w >> 1, ks = w & 1;

  STAMP_RT(12);
  STAMP_AT(0);

  asm volatile("" ::"s"(idx), "s"(table), "s"(xw), "s"(ww), "s"(bw), "s"(W1), "s"(b1), "s"(W2), "s"(b2), "s"(out), "s"(ws));
  unsigned warm = 0, warm2 = 0;
  {
    const int e3 = mt * 94 + ((tid >> 1) < 94 ? (tid >> 1) : 93), ln = e3 < 3000 ? e3 : 2999;
    const unsigned short* p2 = table + (size_t)(ln / 3) * D_ + (1 + ln % 3) * 32;
    const int t = tid < 208 ? tid : 207;
    const unsigned short* p = W1 + (size_t)(mt * 104 + (t >> 1)) * H_ + n0 + (t & 1) * 32;
    asm volatile("global_load_dword %0, %2, off\n\tglobal_load_dword %1, %3, off"
                 : "=&v"(warm2), "=&v"(warm) : "v"(p2), "v"(p) : "memory");
  }
  unsigned epoch, wv_[2] = {0, 0}, bwv = 0, b2v = 0;
#define LDH(dst, ptr) asm volatile("global_load_ushort %0, %1, off" : "=v"(dst) : "v"(ptr) : "memory")
  asm volatile("global_load_dword %0, %1, off sc1" : "=v"(epoch) : "v"(ws + mt * 128) : "memory");
  if (w == 0) {
    const int g = lane >> 3, g2 = g + 8 < ND_ ? g + 8 : ND_ - 1;
    LDH(wv_[0], ww + g); LDH(wv_[1], ww + g2); LDH(bwv, bw); LDH(b2v, b2);
  }
#undef LDH
  Ctx c;
  const __amdgpu_buffer_rsrc_t srdI = __builtin_amdgcn_make_buffer_rsrc((void*)idx, (short)0, B_ * F_ * 4, 0x00020000);
  {
    const unsigned vI = (unsigned)lane * 16u, p3 = w == 0 ? 12u : (unsigned)w + 8u;
    dma1<65536u, 0, 0>((unsigned)w * 1024u, vI, srdI, (unsigned)(m0 * F_ * 4 + w * 1024));
    dma1<65536u + 4096u, 4096u, 0>((unsigned)w * 1024u, vI, srdI, (unsigned)(m0 * F_ * 4 + w * 1024));
    dma1<65536u, 0, 0>(p3 * 1024u, vI, srdI, (unsigned)(m0 * F_ * 4) + p3 * 1024u);
    dma1<65536u, 0, 0>((w == 0 ? 8u : 12u) * 1024u, vI, srdI, (unsigned)(m0 * F_ * 4) + (w == 0 ? 8u : 12u) * 1024u);
  }
  c.srdW = __builtin_amdgcn_make_buffer_rsrc((void*)W1, (short)0, WREC, 0x00020000);
  c.srdT = __builtin_amdgcn_make_buffer_rsrc((void*)table, (short)0, TREC, 0x00020000);
  c.ring = RING0 + w * 8192;
  c.wbase = (unsigned)(fh * 13 * 128 + ks * 16) * 1024u;
  c.tgrp = (unsigned)w * 4096u;
  c.vT[0] = (unsigned)(lane >> 2) * 256u + (unsigned)(((lane & 3) ^ ((lane >> 4) & 3)) * 16);
  c.vT[1] = c.vT[0] + 3072u; c.vT[2] = c.vT[0] + 6144u; c.vT[3] = c.vT[0] + 9216u;
  c.vW0 = (unsigned)(lane >> 3) * 1024u + (unsigned)(((lane & 7) ^ (((lane >> 4) & 1) << 2)) * 16) + (unsigned)n0 * 2u;
  c.vW1 = c.vW0 + 7168u;
  {
    const int qq = (lane & 15) >> 2, p = lane & 3, swz = (qq >> 1) & 1;
    const unsigned base = c.ring + (unsigned)((8 * (lane >> 5) + qq) * 128 + ((lane >> 4) & 1) * 32 + p * 8);
    c.tb0 = base + (unsigned)(swz * 64);
    c.tb1 = base + (unsigned)((swz ^ 1) * 64);
  }
  STAMP_AT(13);
  asm volatile("" ::: ACLOB);
  const unsigned tq0 = c.tgrp * 4u;
  dmaT<0>(c, c.tgrp, tq0); dmaT<1>(c, c.tgrp, tq0); Zero<0, 16>::run();
  dma1<0, 0, 0>(c.ring, c.vW0, c.srdW, c.wbase); dma1<0, 0, 1024>(c.ring, c.vW1, c.srdW, c.wbase); Zero<16, 16>::run();
  dmaT<2>(c, c.tgrp, tq0); dmaT<3>(c, c.tgrp, tq0); Zero<32, 16>::run();
  dmaT<4>(c, c.tgrp, tq0); dmaT<5>(c, c.tgrp, tq0); Zero<48, 16>::run();
  dmaT<6>(c, c.tgrp, tq0); dmaT<7>(c, c.tgrp, tq0); Zero<64, 16>::run();
  STAMP_AT(14);
  waitV<10>();
  bar();
  STAMP_AT(15);
  unsigned warm3 = 0;
  asm volatile("global_load_dword %0, %1, off" : "=v"(warm3) : "v"(out + m0 + nt * 16) : "memory");
  {
    const unsigned cs = (unsigned)(ks * 2 + (lane >> 5)) << 4;
#define ADDR_BLOCK(i)                                                                          \
    {                                                                                          \
      const int row = ((w + i) & 3) * 32 + (lane & 31);                                        \
      const int* ip = (const int*)(lds + 65536 + row * (F_ * 4) + fh * 52);                    \
      unsigned v[13];                                                                          \
      _Pragma("unroll") for (int j = 0; j < 13; ++j) v[j] = (unsigned)ip[j];                   \
      dmaT<8 + 2 * i>(c, c.tgrp, tq0); dmaT<9 + 2 * i>(c, c.tgrp, tq0);                  \
      Zero<80 + 12 * i, 12>::run();                                                            \
      _Pragma("unroll") for (int j = 0; j < 13; ++j)                                           \
        c.addr[j][i] = (v[j] << 6) | (((v[j] << 2) & 0x30u) ^ cs);                             \
    }
    ADDR_BLOCK(0) ADDR_BLOCK(1) ADDR_BLOCK(2) ADDR_BLOCK(3)
#undef ADDR_BLOCK
  }
  STAMP_AT(16);
  dma1<2048, 131072, 0>(c.ring, c.vW0, c.srdW, c.wbase); dma1<2048, 131072, 1024>(c.ring, c.vW1, c.srdW, c.wbase);
  {
    const unsigned char* src = lane >= 16 && lane < 42 ? (const unsigned char*)(xw + (size_t)(m0 + nt * 16) * ND_) + (lane - 16) * 16
                               : (const unsigned char*)((lane >> 3) == 1 ? W2 : b1) + n0 * 2 + (lane & 7) * 16;
    asm volatile("s_mov_b32 m0, %0\n\ts_nop 0\n\tglobal_load_lds_dwordx4 %1, off" ::"s"(SMALL0), "v"(src) : "memory");
  }
  dma1<4096, 2 * 131072, 0>(c.ring, c.vW0, c.srdW, c.wbase); dma1<4096, 2 * 131072, 1024>(c.ring, c.vW1, c.srdW, c.wbase);
  dma1<6144, 3 * 131072, 0>(c.ring, c.vW0, c.srdW, c.wbase); dma1<6144, 3 * 131072, 1024>(c.ring, c.vW1, c.srdW, c.wbase);
  STAMP_AT(1);
  waitL<0>();
  waitV<2 + 1 + 4>();
  asm volatile("" ::"v"(warm), "v"(warm2), "v"(warm3));
  bar();
  STAMP_AT(3);
  readW<regW(2, 0), 0>(c.tb0);
  readW<regW(2, 1), 0>(c.tb1);
  readE<regE(2, 0)>(c.addr[0][0]); readE<regE(2, 1)>(c.addr[0][1]);
  readE<regE(2, 2)>(c.addr[0][2]); readE<regE(2, 3)>(c.addr[0][3]);

  unsigned slot = 0;
#pragma nounroll
  for (int q = 0; q < 3; ++q) {
    phase<false>(c, q, slot);
  }
  u32x2 b1v[8], w2v[8];
  unsigned xv[4];
  {
    const unsigned char* sp = lds + SMALL0 + (lane >> 5) * 8;
#pragma unroll
    for (int g = 0; g < 8; ++g) { b1v[g] = *(const u32x2*)(sp + g * 16); w2v[g] = *(const u32x2*)(sp + 128 + g * 16); }
    const int g = lane >> 3, g2 = g + 8 < ND_ ? g + 8 : ND_ - 1;
    const unsigned short* xp = (const unsigned short*)(lds + SMALL0 + 256) + (lane & 7) * 2 * ND_;
    xv[0] = xp[g]; xv[1] = xp[ND_ + g]; xv[2] = xp[g2]; xv[3] = xp[ND_ + g2];
  }
  phase<true>(c, 3, slot);
  STAMP_AT(7);

#pragma unroll
  for (int g = 0; g < 8; ++g) asm volatile("" : "+v"(b1v[g]), "+v"(w2v[g]));
  asm volatile("s_nop 7\n\ts_waitcnt vmcnt(0) lgkmcnt(0)\n\ts_barrier"
               : "+v"(xv[0]), "+v"(xv[1]), "+v"(xv[2]), "+v"(xv[3]), "+v"(wv_[0]), "+v"(wv_[1]), "+v"(bwv), "+v"(b2v), "+v"(epoch)
               :: "memory");
  f32x4 h[8], w2f[8];
  const unsigned s1 = (unsigned)(RED0 + (((w + 1) & 3) * 3 + 0) * 8192 + lane * 16);
  const unsigned s2 = (unsigned)(RED0 + (((w + 2) & 3) * 3 + 1) * 8192 + lane * 16);
  const unsigned s3 = (unsigned)(RED0 + (((w + 3) & 3) * 3 + 2) * 8192 + lane * 16);
#define SEND(g)                                                                                                   \
  writeAcc<32 + g * 4, g * 1024>(s1);                                                                             \
  h[g] = readAcc4<g>();                                                                                           \
  h[g].x += __uint_as_float(b1v[g].x << 16); h[g].y += __uint_as_float(b1v[g].x & 0xffff0000u);                   \
  asm volatile("" : "+v"(h[g]));                                                                                  \
  writeAcc<64 + g * 4, g * 1024>(s2);                                                                             \
  h[g].z += __uint_as_float(b1v[g].y << 16); h[g].w += __uint_as_float(b1v[g].y & 0xffff0000u);                   \
  w2f[g].x = __uint_as_float(w2v[g].x << 16); w2f[g].y = __uint_as_float(w2v[g].x & 0xffff0000u);                 \
  asm volatile("" : "+v"(h[g]), "+v"(w2f[g].x), "+v"(w2f[g].y));                                                  \
  writeAcc<96 + g * 4, g * 1024>(s3);                                                                             \
  w2f[g].z = __uint_as_float(w2v[g].y << 16); w2f[g].w = __uint_as_float(w2v[g].y & 0xffff0000u);                 \
  asm volatile("" : "+v"(w2f[g].z), "+v"(w2f[g].w));
  SEND(0) SEND(1) SEND(2) SEND(3) SEND(4) SEND(5) SEND(6) SEND(7)
#undef SEND
  float wide = __uint_as_float(xv[0] << 16) * __uint_as_float(wv_[0] << 16), wide1 = __uint_as_float(xv[1] << 16) * __uint_as_float(wv_[0] << 16);
  if ((lane >> 3) + 8 < ND_) {
    wide += __uint_as_float(xv[2] << 16) * __uint_as_float(wv_[1] << 16);
    wide1 += __uint_as_float(xv[3] << 16) * __uint_as_float(wv_[1] << 16);
  }
  wide = sumX32(sumX16(sumX8(wide))); wide1 = sumX32(sumX16(sumX8(wide1)));
  asm volatile("s_waitcnt lgkmcnt(0)\n\ts_barrier" : "+v"(wide), "+v"(wide1) :: "memory");
  STAMP_AT(8);
  f32x4 r[24];
#pragma unroll
  for (int g = 0; g < 8; ++g) {
    const unsigned char* p = lds + RED0 + w * 3 * 8192 + g * 1024 + lane * 16;
    r[g * 3] = *(const f32x4*)p; r[g * 3 + 1] = *(const f32x4*)(p + 8192); r[g * 3 + 2] = *(const f32x4*)(p + 16384);
  }
  f32x4 pacc = {0.f, 0.f, 0.f, 0.f};
#pragma unroll
  for (int g = 0; g < 8; ++g) {
    if ((g & 1) == 0)
      asm volatile("" : "+v"(r[g * 3]), "+v"(r[g * 3 + 1]), "+v"(r[g * 3 + 2]), "+v"(r[g * 3 + 3]), "+v"(r[g * 3 + 4]), "+v"(r[g * 3 + 5]) :: "memory");
    const f32x4 s = (h[g] + r[g * 3]) + (r[g * 3 + 1] + r[g * 3 + 2]);
    pacc.x = fmaf(fmaxf(s.x, 0.f), w2f[g].x, pacc.x); pacc.y = fmaf(fmaxf(s.y, 0.f), w2f[g].y, pacc.y);
    pacc.z = fmaf(fmaxf(s.z, 0.f), w2f[g].z, pacc.z); pacc.w = fmaf(fmaxf(s.w, 0.f), w2f[g].w, pacc.w);
  }
  const float part = sumX32((pacc.x + pacc.y) + (pacc.z + pacc.w));

  const unsigned tag = epoch + 1u;
  gu64* gran = (gu64*)(ws + WS_GRAN);
  if (lane < 32)
    __hip_atomic_store(gran + (size_t)((mt * 8 + nt) * 128 + w * 32 + lane), ((unsigned long long)tag << 32) | __float_as_uint(part),
                       __ATOMIC_RELAXED, __HIP_MEMORY_SCOPE_AGENT);
  STAMP_AT(9);
  if (w == 0) {
    const unsigned char* gp = ws + WS_GRAN + (size_t)((mt * 8 + (lane >> 3)) * 128 + nt * 16 + (lane & 7) * 2) * 8;
    u32x4 gv;
#pragma unroll 1
    for (unsigned spins = 0; spins < (1u << 22); ++spins) {
      asm volatile("global_load_dwordx4 %0, %1, off sc1\n\ts_waitcnt vmcnt(0)" : "=v"(gv) : "v"(gp) : "memory");
      if (__all(gv.y == tag && gv.w == tag)) break;
    }
    float d0 = __uint_as_float(gv.x), d1 = __uint_as_float(gv.z);
    d0 = sumX32(sumX16(sumX8(d0))); d1 = sumX32(sumX16(sumX8(d1)));
    if (lane < 8) {
      const float bwf = __uint_as_float(bwv << 16), b2f = __uint_as_float(b2v << 16);
      const float z0 = rbf(rbf(rbf(wide) + bwf) + rbf(rbf(d0) + b2f)), z1 = rbf(rbf(rbf(wide1) + bwf) + rbf(rbf(d1) + b2f));
      __hip_bfloat162 o;
      o.x = __float2bfloat16(1.f / (1.f + __expf(-z0)));
      o.y = __float2bfloat16(1.f / (1.f + __expf(-z1)));
      *(__hip_bfloat162*)(out + m0 + nt * 16 + lane * 2) = o;
    }
    if (nt == 0 && lane == 0) __hip_atomic_store((gu32*)(ws + mt * 128), tag, __ATOMIC_RELAXED, __HIP_MEMORY_SCOPE_AGENT);
  }
}

extern "C" void kernel_launch(void* const* d_in, const int* in_sizes, int n_in, void* d_out, int out_size, void* d_ws,
                              size_t ws_size, hipStream_t stream) {
  (void)in_sizes; (void)n_in; (void)out_size;
  if (ws_size < (size_t)WS_GRAN + 32 * 8 * 128 * 8) return;
  (void)hipFuncSetAttribute((const void*)wide_deep_kernel, hipFuncAttributeMaxDynamicSharedMemorySize, LDS_BYTES);
  wide_deep_kernel<<<256, 256, LDS_BYTES, stream>>>(
      (const int*)d_in[0], (const unsigned short*)d_in[1], (const unsigned short*)d_in[2], (const unsigned short*)d_in[3],
      (const unsigned short*)d_in[4], (const unsigned short*)d_in[5], (const unsigned short*)d_in[6],
      (const unsigned short*)d_in[7], (const unsigned short*)d_in[8], (__hip_bfloat16*)d_out, (unsigned char*)d_ws);
}
```

```cpp
#include <hip/hip_runtime.h>
#include <hip/hip_bf16.h>
#include <stdint.h>

typedef unsigned u32x4 __attribute__((ext_vector_type(4)));
typedef unsigned u32x2 __attribute__((ext_vector_type(2)));
typedef float f32x4 __attribute__((ext_vector_type(4)));
typedef __attribute__((address_space(1))) unsigned long long gu64;
typedef __attribute__((address_space(1))) unsigned gu32;

constexpr int B_ = 4096, F_ = 26, D_ = 128, V_ = 1000, ND_ = 13, H_ = 512;
constexpr int LDS_BYTES = 163840;
constexpr int RING0 = 131072;
constexpr int SMALL0 = 64512;
constexpr int RED0 = 65536;
constexpr int WS_GRAN = 4096;

#define WREC (F_ * D_ * H_ * 2)
#define TREC (V_ * D_ * 2)
#define ACLOB "a0","a1","a2","a3","a4","a5","a6","a7","a8","a9","a10","a11","a12","a13","a14","a15", \
  "a16","a17","a18","a19","a20","a21","a22","a23","a24","a25","a26","a27","a28","a29","a30","a31", \
  "a32","a33","a34","a35","a36","a37","a38","a39","a40","a41","a42","a43","a44","a45","a46","a47", \
  "a48","a49","a50","a51","a52","a53","a54","a55","a56","a57","a58","a59","a60","a61","a62","a63", \
  "a64","a65","a66","a67","a68","a69","a70","a71","a72","a73","a74","a75","a76","a77","a78","a79", \
  "a80","a81","a82","a83","a84","a85","a86","a87","a88","a89","a90","a91","a92","a93","a94","a95", \
  "a96","a97","a98","a99","a100","a101","a102","a103","a104","a105","a106","a107","a108","a109","a110","a111", \
  "a112","a113","a114","a115","a116","a117","a118","a119","a120","a121","a122","a123","a124","a125","a126","a127", \
  "a128","a129","a130","a131","a132","a133","a134","a135","a136","a137","a138","a139","a140","a141","a142","a143", \
  "a144","a145","a146","a147","a148","a149","a150","a151","a152","a153","a154","a155","a156","a157","a158","a159", \
  "a160","a161","a162","a163","a164","a165","a166","a167","a168","a169","a170","a171","a172","a173","a174","a175", \
  "a176","a177","a178","a179","a180","a181","a182","a183","a184","a185","a186","a187","a188","a189","a190","a191", \
  "a192","a193","a194","a195","a196","a197","a198","a199"

struct Ctx {
  unsigned addr[13][4];
  unsigned tb0, tb1;
  unsigned vW0, vW1;
  unsigned vT[4];
  __amdgpu_buffer_rsrc_t srdW, srdT;
  unsigned ring;
  unsigned wbase;
  unsigned tgrp;
};

__host__ __device__ constexpr int setOf(int j) { return j == 0 ? 2 : (j & 1); }
__host__ __device__ constexpr int regE(int set, int i) { return 128 + set * 24 + i * 4; }
__host__ __device__ constexpr int regW(int set, int nb) { return 128 + set * 24 + 16 + nb * 4; }
__host__ __device__ constexpr int regC(int i, int nb) { return (i * 2 + nb) * 16; }

#define STAMP_AT(i) do {} while (0)
#define STAMP_RT(i) do {} while (0)
template <int N> __device__ __forceinline__ void waitV() { asm volatile("s_waitcnt vmcnt(%0)" ::"n"(N) : "memory"); }
template <int N> __device__ __forceinline__ void waitL() { asm volatile("s_waitcnt lgkmcnt(%0)" ::"n"(N) : "memory"); }
__device__ __forceinline__ void bar() { asm volatile("s_barrier" ::: "memory"); }

template <int R> __device__ __forceinline__ void readE(unsigned& a) {
  asm volatile("ds_read_b128 a[%c1:%c2], %0\n\tv_xor_b32 %0, 0x10000, %0" : "+v"(a) : "i"(R), "i"(R + 3) : "memory");
}
template <int R, int OFF> __device__ __forceinline__ void readW(unsigned a) {
  asm volatile("ds_read_b64_tr_b16 a[%c1:%c2], %0 offset:%c3\n\tds_read_b64_tr_b16 a[%c4:%c5], %0 offset:%c6"
               ::"v"(a), "i"(R), "i"(R + 1), "i"(OFF), "i"(R + 2), "i"(R + 3), "i"(OFF + 512) : "memory");
}
template <int C, int W, int E> __device__ __forceinline__ void mfma() {
  asm volatile("v_mfma_f32_32x32x16_bf16 a[%c0:%c1], a[%c2:%c3], a[%c4:%c5], a[%c0:%c1]"
               ::"i"(C), "i"(C + 15), "i"(W), "i"(W + 3), "i"(E), "i"(E + 3));
}
template <unsigned M, unsigned SO, int OFF>
__device__ __forceinline__ void dma1(unsigned m0a, unsigned voff, __amdgpu_buffer_rsrc_t srd, unsigned soa) {
  unsigned so;
  asm volatile("s_add_u32 m0, %1, %5\n\ts_add_u32 %0, %4, %6\n\tbuffer_load_dwordx4 %2, %3, %0 offen offset:%c7 lds"
               : "=&s"(so) : "s"(m0a), "v"(voff), "s"(srd), "s"(soa), "i"(M), "i"(SO), "i"(OFF) : "memory", "scc");
}

__host__ __device__ constexpr int nS(int j, bool last) { return (last || j < 0 || j > 10) ? 0 : (j == 10 ? 1 : 2 - (j & 1)); }
__host__ __device__ constexpr int firstS(int j) { int n = 0; for (int i = 0; i < j; ++i) n += nS(i, false); return n; }
__host__ __device__ constexpr int nB(int j, bool last) { return (!last || j <= 8) ? 2 : 0; }
__host__ __device__ constexpr int opsAll(int j, bool last) { return j < 0 ? 2 + nS(j + 13, false) : nB(j, last) + nS(j, last); }

template <int K> __device__ __forceinline__ void dmaT(const Ctx& c, unsigned m0a, unsigned soa) {
  constexpr int g = K >> 2, sub = K & 3;
  if constexpr (K == 15) m0a = c.tgrp == 3 * 4096u ? 130048u - 52224u : m0a;
  dma1<g * 16384u, g * 65536u, sub * 1024>(m0a, c.vT[sub], c.srdT, soa);
}

template <int J, bool LAST>
__device__ __forceinline__ void step(Ctx& c, unsigned wq, unsigned lb, unsigned tq, unsigned& slot) {
  constexpr int cur = setOf(J), JN = (J + 1) % 13, nxt = setOf(JN);
  constexpr bool hasNext = !(LAST && J == 12);
  constexpr int ns = nS(J, LAST), k0 = firstS(J);
  constexpr bool doW = nB(J, LAST) != 0;
  constexpr unsigned WO = (unsigned)((J + 4) % 13) * 131072u + ((J + 4) >= 13 ? 32768u : 0u);
  const unsigned slotN = (slot + 2048) & 8191;
  if constexpr (J == 12 && !LAST) { waitV<4>(); waitL<0>(); bar(); }
  else { if constexpr (hasNext) waitV<opsAll(J - 2, LAST) + opsAll(J - 1, LAST)>(); waitL<3>(); }
  const unsigned t0 = c.tb0 + slotN, t1 = c.tb1 + slotN, ms = c.ring + slot;
  mfma<regC(0, 0), regW(cur, 0), regE(cur, 0)>();
  if constexpr (hasNext) readW<regW(nxt, 0), 0>(t0);
  mfma<regC(0, 1), regW(cur, 1), regE(cur, 0)>();
  if constexpr (hasNext) readW<regW(nxt, 1), 0>(t1);
  if constexpr (hasNext) waitL<6>(); else waitL<2>();
  mfma<regC(1, 0), regW(cur, 0), regE(cur, 1)>();
  if constexpr (hasNext) readE<regE(nxt, 0)>(c.addr[JN][0]);
  if constexpr (ns >= 1) dmaT<k0>(c, lb, tq);
  mfma<regC(1, 1), regW(cur, 1), regE(cur, 1)>();
  if constexpr (doW) dma1<0, WO, 0>(ms, c.vW0, c.srdW, wq);
  if constexpr (hasNext) waitL<6>(); else waitL<1>();
  mfma<regC(2, 0), regW(cur, 0), regE(cur, 2)>();
  if constexpr (hasNext) readE<regE(nxt, 1)>(c.addr[JN][1]);
  if constexpr (ns >= 2) dmaT<k0 + 1>(c, lb, tq);
  mfma<regC(2, 1), regW(cur, 1), regE(cur, 2)>();
  if constexpr (doW) dma1<0, WO, 1024>(ms, c.vW1, c.srdW, wq);
  if constexpr (hasNext) waitL<6>(); else waitL<0>();
  mfma<regC(3, 0), regW(cur, 0), regE(cur, 3)>();
  if constexpr (hasNext) readE<regE(nxt, 2)>(c.addr[JN][2]);
  mfma<regC(3, 1), regW(cur, 1), regE(cur, 3)>();
  if constexpr (hasNext) readE<regE(nxt, 3)>(c.addr[JN][3]);
  slot = slotN;
}

template <bool LAST> __device__ __forceinline__ void phase(Ctx& c, int q, unsigned& slot) {
  const unsigned wq = c.wbase + (unsigned)q * 32768u, lb = (unsigned)(((q + 1) & 1) << 16) + c.tgrp,
                 tq = c.tgrp * 4u + (unsigned)(q + 1) * 64u;
  step<0, LAST>(c, wq, lb, tq, slot); step<1, LAST>(c, wq, lb, tq, slot); step<2, LAST>(c, wq, lb, tq, slot); step<3, LAST>(c, wq, lb, tq, slot);
  step<4, LAST>(c, wq, lb, tq, slot); step<5, LAST>(c, wq, lb, tq, slot); step<6, LAST>(c, wq, lb, tq, slot); step<7, LAST>(c, wq, lb, tq, slot);
  step<8, LAST>(c, wq, lb, tq, slot); step<9, LAST>(c, wq, lb, tq, slot); step<10, LAST>(c, wq, lb, tq, slot); step<11, LAST>(c, wq, lb, tq, slot);
  step<12, LAST>(c, wq, lb, tq, slot);
}

template <int R> __device__ __forceinline__ void zero1() { asm volatile("v_accvgpr_write_b32 a[%c0], 0" ::"i"(R)); }
template <int R, int N> struct Zero { static __device__ __forceinline__ void run() { zero1<R>(); Zero<R + 1, N - 1>::run(); } };
template <int R> struct Zero<R, 0> { static __device__ __forceinline__ void run() {} };

template <int R, int OFF> __device__ __forceinline__ void writeAcc(unsigned a) {
  asm volatile("ds_write_b128 %0, a[%c1:%c2] offset:%c3" ::"v"(a), "i"(R), "i"(R + 3), "i"(OFF) : "memory");
}
template <int R> __device__ __forceinline__ float readAcc() {
  float v; asm volatile("v_accvgpr_read_b32 %0, a[%c1]" : "=v"(v) : "i"(R)); return v;
}
template <int G> __device__ __forceinline__ f32x4 readAcc4() {
  f32x4 v; v.x = readAcc<G * 4>(); v.y = readAcc<G * 4 + 1>(); v.z = readAcc<G * 4 + 2>(); v.w = readAcc<G * 4 + 3>(); return v;
}

__device__ __forceinline__ float sumX8(float x) {
  return x + __int_as_float(__builtin_amdgcn_update_dpp(0, __float_as_int(x), 0x128, 0xf, 0xf, true));
}
__device__ __forceinline__ float sumX16(float x) {
  const auto r = __builtin_amdgcn_permlane16_swap(__float_as_uint(x), __float_as_uint(x), false, false);
  return __uint_as_float(r[0]) + __uint_as_float(r[1]);
}
__device__ __forceinline__ float sumX32(float x) {
  const auto r = __builtin_amdgcn_permlane32_swap(__float_as_uint(x), __float_as_uint(x), false, false);
  return __uint_as_float(r[0]) + __uint_as_float(r[1]);
}
__device__ __forceinline__ float rbf(float x) { return __bfloat162float(__float2bfloat16(x)); }

__global__ __launch_bounds__(256, 1) void wide_deep_kernel(
    const int* __restrict__ idx, const unsigned short* __restrict__ table, const unsigned short* __restrict__ xw,
    const unsigned short* __restrict__ ww, const unsigned short* __restrict__ bw, const unsigned short* __restrict__ W1,
    const unsigned short* __restrict__ b1, const unsigned short* __restrict__ W2, const unsigned short* __restrict__ b2,
    __hip_bfloat16* __restrict__ out, unsigned char* ws) {
  extern __shared__ __attribute__((aligned(16))) unsigned char lds[];
  const int tid = threadIdx.x, lane = tid & 63;
  const int w = __builtin_amdgcn_readfirstlane(tid >> 6);
  const int nt = blockIdx.x & 7, mt = blockIdx.x >> 3;
  const int n0 = nt * 64, m0 = mt * 128;
  const int fh = w >> 1, ks = w & 1;

  STAMP_RT(12);
  STAMP_AT(0);

  asm volatile("" ::"s"(idx), "s"(table), "s"(xw), "s"(ww), "s"(bw), "s"(W1), "s"(b1), "s"(W2), "s"(b2), "s"(out), "s"(ws));
  unsigned warm = 0, warm2 = 0;
  {
    const int e3 = mt * 94 + ((tid >> 1) < 94 ? (tid >> 1) : 93), ln = e3 < 3000 ? e3 : 2999;
    const unsigned short* p2 = table + (size_t)(ln / 3) * D_ + (1 + ln % 3) * 32;
    const int t = tid < 208 ? tid : 207;
    const unsigned short* p = W1 + (size_t)(mt * 104 + (t >> 1)) * H_ + n0 + (t & 1) * 32;
    asm volatile("global_load_dword %0, %2, off\n\tglobal_load_dword %1, %3, off"
                 : "=&v"(warm2), "=&v"(warm) : "v"(p2), "v"(p) : "memory");
  }
  unsigned epoch, wv_[2] = {0, 0}, bwv = 0, b2v = 0, warm3 = 0;
#define LDH(dst, ptr) asm volatile("global_load_ushort %0, %1, off" : "=v"(dst) : "v"(ptr) : "memory")
  asm volatile("global_load_dword %0, %1, off sc1" : "=v"(epoch) : "v"(ws + mt * 128) : "memory");
  if (w == 0) {
    const int g = lane >> 3, g2 = g + 8 < ND_ ? g + 8 : ND_ - 1;
    LDH(wv_[0], ww + g); LDH(wv_[1], ww + g2); LDH(bwv, bw); LDH(b2v, b2);
    LDH(warm3, out + m0 + nt * 16);
  }
#undef LDH
  Ctx c;
  const __amdgpu_buffer_rsrc_t srdI = __builtin_amdgcn_make_buffer_rsrc((void*)idx, (short)0, B_ * F_ * 4, 0x00020000);
  {
    const unsigned vI = (unsigned)lane * 16u, p3 = w == 0 ? 12u : (unsigned)w + 8u;
    dma1<65536u, 0, 0>((unsigned)w * 1024u, vI, srdI, (unsigned)(m0 * F_ * 4 + w * 1024));
    dma1<65536u + 4096u, 4096u, 0>((unsigned)w * 1024u, vI, srdI, (unsigned)(m0 * F_ * 4 + w * 1024));
    dma1<65536u, 0, 0>(p3 * 1024u, vI, srdI, (unsigned)(m0 * F_ * 4) + p3 * 1024u);
    dma1<65536u, 0, 0>((w == 0 ? 8u : 12u) * 1024u, vI, srdI, (unsigned)(m0 * F_ * 4) + (w == 0 ? 8u : 12u) * 1024u);
  }
  {
    const unsigned char* src = lane >= 16 && lane < 42 ? (const unsigned char*)(xw + (size_t)(m0 + nt * 16) * ND_) + (lane - 16) * 16
                               : (const unsigned char*)((lane >> 3) == 1 ? W2 : b1) + n0 * 2 + (lane & 7) * 16;
    asm volatile("s_mov_b32 m0, %0\n\ts_nop 0\n\tglobal_load_lds_dwordx4 %1, off" ::"s"(SMALL0), "v"(src) : "memory");
  }
  c.srdW = __builtin_amdgcn_make_buffer_rsrc((void*)W1, (short)0, WREC, 0x00020000);
  c.srdT = __builtin_amdgcn_make_buffer_rsrc((void*)table, (short)0, TREC, 0x00020000);
  c.ring = RING0 + w * 8192;
  c.wbase = (unsigned)(fh * 13 * 128 + ks * 16) * 1024u;
  c.tgrp = (unsigned)w * 4096u;
  c.vT[0] = (unsigned)(lane >> 2) * 256u + (unsigned)(((lane & 3) ^ ((lane >> 4) & 3)) * 16);
  c.vT[1] = c.vT[0] + 3072u; c.vT[2] = c.vT[0] + 6144u; c.vT[3] = c.vT[0] + 9216u;
  c.vW0 = (unsigned)(lane >> 3) * 1024u + (unsigned)(((lane & 7) ^ (((lane >> 4) & 1) << 2)) * 16) + (unsigned)n0 * 2u;
  c.vW1 = c.vW0 + 7168u;
  {
    const int qq = (lane & 15) >> 2, p = lane & 3, swz = (qq >> 1) & 1;
    const unsigned base = c.ring + (unsigned)((8 * (lane >> 5) + qq) * 128 + ((lane >> 4) & 1) * 32 + p * 8);
    c.tb0 = base + (unsigned)(swz * 64);
    c.tb1 = base + (unsigned)((swz ^ 1) * 64);
  }
  STAMP_AT(13);
  asm volatile("" ::: ACLOB);
  const unsigned tq0 = c.tgrp * 4u;
  dmaT<0>(c, c.tgrp, tq0); dmaT<1>(c, c.tgrp, tq0); Zero<0, 16>::run();
  dma1<0, 0, 0>(c.ring, c.vW0, c.srdW, c.wbase); dma1<0, 0, 1024>(c.ring, c.vW1, c.srdW, c.wbase); Zero<16, 16>::run();
  dmaT<2>(c, c.tgrp, tq0); dmaT<3>(c, c.tgrp, tq0); Zero<32, 16>::run();
  dmaT<4>(c, c.tgrp, tq0); dmaT<5>(c, c.tgrp, tq0); Zero<48, 16>::run();
  dmaT<6>(c, c.tgrp, tq0); dmaT<7>(c, c.tgrp, tq0); Zero<64, 16>::run();
  STAMP_AT(14);
  waitV<11>();
  bar();
  STAMP_AT(15);
  {
    const unsigned cs = (unsigned)(ks * 2 + (lane >> 5)) << 4;
#define ADDR_BLOCK(i)                                                                          \
    {                                                                                          \
      const int row = ((w + i) & 3) * 32 + (lane & 31);                                        \
      const int* ip = (const int*)(lds + 65536 + row * (F_ * 4) + fh * 52);                    \
      unsigned v[13];                                                                          \
      _Pragma("unroll") for (int j = 0; j < 13; ++j) v[j] = (unsigned)ip[j];                   \
      dmaT<8 + 2 * i>(c, c.tgrp, tq0); dmaT<9 + 2 * i>(c, c.tgrp, tq0);                  \
      Zero<80 + 12 * i, 12>::run();                                                            \
      _Pragma("unroll") for (int j = 0; j < 13; ++j)                                           \
        c.addr[j][i] = (v[j] << 6) | (((v[j] << 2) & 0x30u) ^ cs);                             \
    }
    ADDR_BLOCK(0) ADDR_BLOCK(1) ADDR_BLOCK(2) ADDR_BLOCK(3)
#undef ADDR_BLOCK
  }
  STAMP_AT(16);
  dma1<2048, 131072, 0>(c.ring, c.vW0, c.srdW, c.wbase); dma1<2048, 131072, 1024>(c.ring, c.vW1, c.srdW, c.wbase);
  dma1<4096, 2 * 131072, 0>(c.ring, c.vW0, c.srdW, c.wbase); dma1<4096, 2 * 131072, 1024>(c.ring, c.vW1, c.srdW, c.wbase);
  dma1<6144, 3 * 131072, 0>(c.ring, c.vW0, c.srdW, c.wbase); dma1<6144, 3 * 131072, 1024>(c.ring, c.vW1, c.srdW, c.wbase);
  STAMP_AT(1);
  waitL<0>();
  waitV<2 + 4>();
  asm volatile("" ::"v"(warm), "v"(warm2), "v"(warm3));
  bar();
  STAMP_AT(3);
  readW<regW(2, 0), 0>(c.tb0);
  readW<regW(2, 1), 0>(c.tb1);
  readE<regE(2, 0)>(c.addr[0][0]); readE<regE(2, 1)>(c.addr[0][1]);
  readE<regE(2, 2)>(c.addr[0][2]); readE<regE(2, 3)>(c.addr[0][3]);

  unsigned slot = 0;
#pragma nounroll
  for (int q = 0; q < 3; ++q) {
    phase<false>(c, q, slot);
  }
  u32x2 b1v[8], w2v[8];
  unsigned xv[4];
  {
    const unsigned char* sp = lds + SMALL0 + (lane >> 5) * 8;
#pragma unroll
    for (int g = 0; g < 8; ++g) { b1v[g] = *(const u32x2*)(sp + g * 16); w2v[g] = *(const u32x2*)(sp + 128 + g * 16); }
    const int g = lane >> 3, g2 = g + 8 < ND_ ? g + 8 : ND_ - 1;
    const unsigned short* xp = (const unsigned short*)(lds + SMALL0 + 256) + (lane & 7) * 2 * ND_;
    xv[0] = xp[g]; xv[1] = xp[ND_ + g]; xv[2] = xp[g2]; xv[3] = xp[ND_ + g2];
  }
  phase<true>(c, 3, slot);
  STAMP_AT(7);

#pragma unroll
  for (int g = 0; g < 8; ++g) asm volatile("" : "+v"(b1v[g]), "+v"(w2v[g]));
  asm volatile("s_nop 7\n\ts_waitcnt vmcnt(0) lgkmcnt(0)\n\ts_barrier"
               : "+v"(xv[0]), "+v"(xv[1]), "+v"(xv[2]), "+v"(xv[3]), "+v"(wv_[0]), "+v"(wv_[1]), "+v"(bwv), "+v"(b2v), "+v"(epoch)
               :: "memory");
  f32x4 h[8], w2f[8];
  const unsigned s1 = (unsigned)(RED0 + (((w + 1) & 3) * 3 + 0) * 8192 + lane * 16);
  const unsigned s2 = (unsigned)(RED0 + (((w + 2) & 3) * 3 + 1) * 8192 + lane * 16);
  const unsigned s3 = (unsigned)(RED0 + (((w + 3) & 3) * 3 + 2) * 8192 + lane * 16);
#define SEND(g)                                                                                                   \
  writeAcc<32 + g * 4, g * 1024>(s1);                                                                             \
  h[g] = readAcc4<g>();                                                                                           \
  h[g].x += __uint_as_float(b1v[g].x << 16); h[g].y += __uint_as_float(b1v[g].x & 0xffff0000u);                   \
  asm volatile("" : "+v"(h[g]));                                                                                  \
  writeAcc<64 + g * 4, g * 1024>(s2);                                                                             \
  h[g].z += __uint_as_float(b1v[g].y << 16); h[g].w += __uint_as_float(b1v[g].y & 0xffff0000u);                   \
  w2f[g].x = __uint_as_float(w2v[g].x << 16); w2f[g].y = __uint_as_float(w2v[g].x & 0xffff0000u);                 \
  asm volatile("" : "+v"(h[g]), "+v"(w2f[g].x), "+v"(w2f[g].y));                                                  \
  writeAcc<96 + g * 4, g * 1024>(s3);                                                                             \
  w2f[g].z = __uint_as_float(w2v[g].y << 16); w2f[g].w = __uint_as_float(w2v[g].y & 0xffff0000u);                 \
  asm volatile("" : "+v"(w2f[g].z), "+v"(w2f[g].w));
  SEND(0) SEND(1) SEND(2) SEND(3) SEND(4) SEND(5) SEND(6) SEND(7)
#undef SEND
  float wide = __uint_as_float(xv[0] << 16) * __uint_as_float(wv_[0] << 16), wide1 = __uint_as_float(xv[1] << 16) * __uint_as_float(wv_[0] << 16);
  if ((lane >> 3) + 8 < ND_) {
    wide += __uint_as_float(xv[2] << 16) * __uint_as_float(wv_[1] << 16);
    wide1 += __uint_as_float(xv[3] << 16) * __uint_as_float(wv_[1] << 16);
  }
  wide = sumX32(sumX16(sumX8(wide))); wide1 = sumX32(sumX16(sumX8(wide1)));
  asm volatile("s_waitcnt lgkmcnt(0)\n\ts_barrier" : "+v"(wide), "+v"(wide1) :: "memory");
  STAMP_AT(8);
  f32x4 r[24];
#pragma unroll
  for (int g = 0; g < 8; ++g) {
    const unsigned char* p = lds + RED0 + w * 3 * 8192 + g * 1024 + lane * 16;
    r[g * 3] = *(const f32x4*)p; r[g * 3 + 1] = *(const f32x4*)(p + 8192); r[g * 3 + 2] = *(const f32x4*)(p + 16384);
  }
  f32x4 pacc = {0.f, 0.f, 0.f, 0.f};
#pragma unroll
  for (int g = 0; g < 8; ++g) {
    if ((g & 1) == 0)
      asm volatile("" : "+v"(r[g * 3]), "+v"(r[g * 3 + 1]), "+v"(r[g * 3 + 2]), "+v"(r[g * 3 + 3]), "+v"(r[g * 3 + 4]), "+v"(r[g * 3 + 5]) :: "memory");
    const f32x4 s = (h[g] + r[g * 3]) + (r[g * 3 + 1] + r[g * 3 + 2]);
    pacc.x = fmaf(fmaxf(s.x, 0.f), w2f[g].x, pacc.x); pacc.y = fmaf(fmaxf(s.y, 0.f), w2f[g].y, pacc.y);
    pacc.z = fmaf(fmaxf(s.z, 0.f), w2f[g].z, pacc.z); pacc.w = fmaf(fmaxf(s.w, 0.f), w2f[g].w, pacc.w);
  }
  const float part = sumX32((pacc.x + pacc.y) + (pacc.z + pacc.w));

  const unsigned tag = epoch + 1u;
  gu64* gran = (gu64*)(ws + WS_GRAN);
  if (lane < 32)
    __hip_atomic_store(gran + (size_t)((mt * 8 + nt) * 128 + w * 32 + lane), ((unsigned long long)tag << 32) | __float_as_uint(part),
                       __ATOMIC_RELAXED, __HIP_MEMORY_SCOPE_AGENT);
  STAMP_AT(9);
  if (w == 0) {
    const unsigned char* gp = ws + WS_GRAN + (size_t)((mt * 8 + (lane >> 3)) * 128 + nt * 16 + (lane & 7) * 2) * 8;
    float d0, d1;
    {
      unsigned long long mk; unsigned n;
#define POLL(a, b, c, d)                                                                                      \
      "s_waitcnt vmcnt(2)\n\tv_cmp_eq_u32 vcc, %5, v" #b "\n\tv_cmp_eq_u32 %2, %5, v" #d "\n\t"           \
      "v_mov_b32 %0, v" #a "\n\tv_mov_b32 %1, v" #c "\n\ts_and_b64 vcc, vcc, %2\n\ts_cmp_eq_u64 vcc, exec\n\t" \
      "s_cbranch_scc1 .Lwd_got\n\tglobal_load_dwordx4 v[" #a ":" #d "], %4, off sc1\n\t"
      asm volatile("global_load_dwordx4 v[240:243], %4, off sc1\n\ts_sleep 8\n\t"
                   "global_load_dwordx4 v[244:247], %4, off sc1\n\ts_sleep 8\n\t"
                   "global_load_dwordx4 v[248:251], %4, off sc1\n\ts_mov_b32 %3, 0x200000\n"
                   ".Lwd_poll:\n\t"
                   POLL(240, 241, 242, 243) POLL(244, 245, 246, 247) POLL(248, 249, 250, 251)
                   "s_sub_u32 %3, %3, 1\n\ts_cmp_lg_u32 %3, 0\n\ts_cbranch_scc1 .Lwd_poll\n"
                   ".Lwd_got:"
                   : "=&v"(d0), "=&v"(d1), "=&s"(mk), "=&s"(n) : "v"(gp), "v"(tag)
                   : "v240", "v241", "v242", "v243", "v244", "v245", "v246", "v247", "v248", "v249", "v250", "v251",
                     "vcc", "scc", "memory");
#undef POLL
    }
    d0 = sumX32(sumX16(sumX8(d0))); d1 = sumX32(sumX16(sumX8(d1)));
    if (lane < 8) {
      const float bwf = __uint_as_float(bwv << 16), b2f = __uint_as_float(b2v << 16);
      const float z0 = rbf(rbf(rbf(wide) + bwf) + rbf(rbf(d0) + b2f)), z1 = rbf(rbf(rbf(wide1) + bwf) + rbf(rbf(d1) + b2f));
      __hip_bfloat162 o;
      o.x = __float2bfloat16(1.f / (1.f + __expf(-z0)));
      o.y = __float2bfloat16(1.f / (1.f + __expf(-z1)));
      *(__hip_bfloat162*)(out + m0 + nt * 16 + lane * 2) = o;
    }
    if (nt == 0 && lane == 0) __hip_atomic_store((gu32*)(ws + mt * 128), tag, __ATOMIC_RELAXED, __HIP_MEMORY_SCOPE_AGENT);
  }
}

extern "C" void kernel_launch(void* const* d_in, const int* in_sizes, int n_in, void* d_out, int out_size, void* d_ws,
                              size_t ws_size, hipStream_t stream) {
  (void)in_sizes; (void)n_in; (void)out_size;
  if (ws_size < (size_t)WS_GRAN + 32 * 8 * 128 * 8) return;
  (void)hipFuncSetAttribute((const void*)wide_deep_kernel, hipFuncAttributeMaxDynamicSharedMemorySize, LDS_BYTES);
  wide_deep_kernel<<<256, 256, LDS_BYTES, stream>>>(
      (const int*)d_in[0], (const unsigned short*)d_in[1], (const unsigned short*)d_in[2], (const unsigned short*)d_in[3],
      (const unsigned short*)d_in[4], (const unsigned short*)d_in[5], (const unsigned short*)d_in[6],
      (const unsigned short*)d_in[7], (const unsigned short*)d_in[8], (__hip_bfloat16*)d_out, (unsigned char*)d_ws);
}
```

```cpp
#include <hip/hip_runtime.h>
#include <hip/hip_bf16.h>
#include <stdint.h>

typedef unsigned u32x4 __attribute__((ext_vector_type(4)));
typedef unsigned u32x2 __attribute__((ext_vector_type(2)));
typedef float f32x4 __attribute__((ext_vector_type(4)));
typedef __attribute__((address_space(1))) unsigned long long gu64;
typedef __attribute__((address_space(1))) unsigned gu32;

constexpr int B_ = 4096, F_ = 26, D_ = 128, V_ = 1000, ND_ = 13, H_ = 512;
constexpr int LDS_BYTES = 163840;
constexpr int RING0 = 131072;
constexpr int SMALL0 = 64512;
constexpr int RED0 = 65536;
constexpr int WS_GRAN = 4096;

constexpr int WREC = F_ * D_ * H_ * 2, TREC = V_ * D_ * 2;
#define ACLOB "a0","a1","a2","a3","a4","a5","a6","a7","a8","a9","a10","a11","a12","a13","a14","a15", \
  "a16","a17","a18","a19","a20","a21","a22","a23","a24","a25","a26","a27","a28","a29","a30","a31", \
  "a32","a33","a34","a35","a36","a37","a38","a39","a40","a41","a42","a43","a44","a45","a46","a47", \
  "a48","a49","a50","a51","a52","a53","a54","a55","a56","a57","a58","a59","a60","a61","a62","a63", \
  "a64","a65","a66","a67","a68","a69","a70","a71","a72","a73","a74","a75","a76","a77","a78","a79", \
  "a80","a81","a82","a83","a84","a85","a86","a87","a88","a89","a90","a91","a92","a93","a94","a95", \
  "a96","a97","a98","a99","a100","a101","a102","a103","a104","a105","a106","a107","a108","a109","a110","a111", \
  "a112","a113","a114","a115","a116","a117","a118","a119","a120","a121","a122","a123","a124","a125","a126","a127", \
  "a128","a129","a130","a131","a132","a133","a134","a135","a136","a137","a138","a139","a140","a141","a142","a143", \
  "a144","a145","a146","a147","a148","a149","a150","a151","a152","a153","a154","a155","a156","a157","a158","a159", \
  "a160","a161","a162","a163","a164","a165","a166","a167","a168","a169","a170","a171","a172","a173","a174","a175", \
  "a176","a177","a178","a179","a180","a181","a182","a183","a184","a185","a186","a187","a188","a189","a190","a191", \
  "a192","a193","a194","a195","a196","a197","a198","a199"

struct Ctx {
  unsigned addr[13][4];
  unsigned tb0, tb1;
  unsigned vW0, vW1;
  unsigned vT[4];
  __amdgpu_buffer_rsrc_t srdW, srdT;
  unsigned ring;
  unsigned wbase;
  unsigned tgrp;
};

__host__ __device__ constexpr int setOf(int j) { return j == 0 ? 2 : (j & 1); }
__host__ __device__ constexpr int regE(int set, int i) { return 128 + set * 24 + i * 4; }
__host__ __device__ constexpr int regW(int set, int nb) { return 128 + set * 24 + 16 + nb * 4; }
__host__ __device__ constexpr int regC(int i, int nb) { return (i * 2 + nb) * 16; }

template <int N> __device__ __forceinline__ void waitV() { asm volatile("s_waitcnt vmcnt(%0)" ::"n"(N) : "memory"); }
template <int N> __device__ __forceinline__ void waitL() { asm volatile("s_waitcnt lgkmcnt(%0)" ::"n"(N) : "memory"); }
__device__ __forceinline__ void bar() { asm volatile("s_barrier" ::: "memory"); }

template <int R> __device__ __forceinline__ void readE(unsigned& a) {
  asm volatile("ds_read_b128 a[%c1:%c2], %0\n\tv_xor_b32 %0, 0x10000, %0" : "+v"(a) : "i"(R), "i"(R + 3) : "memory");
}
template <int R, int OFF> __device__ __forceinline__ void readW(unsigned a) {
  asm volatile("ds_read_b64_tr_b16 a[%c1:%c2], %0 offset:%c3\n\tds_read_b64_tr_b16 a[%c4:%c5], %0 offset:%c6"
               ::"v"(a), "i"(R), "i"(R + 1), "i"(OFF), "i"(R + 2), "i"(R + 3), "i"(OFF + 512) : "memory");
}
template <int C, int W, int E> __device__ __forceinline__ void mfma() {
  asm volatile("v_mfma_f32_32x32x16_bf16 a[%c0:%c1], a[%c2:%c3], a[%c4:%c5], a[%c0:%c1]"
               ::"i"(C), "i"(C + 15), "i"(W), "i"(W + 3), "i"(E), "i"(E + 3));
}
template <unsigned M, unsigned SO, int OFF>
__device__ __forceinline__ void dma1(unsigned m0a, unsigned voff, __amdgpu_buffer_rsrc_t srd, unsigned soa) {
  unsigned so;
  asm volatile("s_add_u32 m0, %1, %5\n\ts_add_u32 %0, %4, %6\n\tbuffer_load_dwordx4 %2, %3, %0 offen offset:%c7 lds"
               : "=&s"(so) : "s"(m0a), "v"(voff), "s"(srd), "s"(soa), "i"(M), "i"(SO), "i"(OFF) : "memory", "scc");
}

__host__ __device__ constexpr int nS(int j, bool last) { return (last || j < 0 || j > 10) ? 0 : (j == 10 ? 1 : 2 - (j & 1)); }
__host__ __device__ constexpr int firstS(int j) { int n = 0; for (int i = 0; i < j; ++i) n += nS(i, false); return n; }
__host__ __device__ constexpr int nB(int j, bool last) { return (!last || j <= 8) ? 2 : 0; }
__host__ __device__ constexpr int opsAll(int j, bool last) { return j < 0 ? 2 + nS(j + 13, false) : nB(j, last) + nS(j, last); }

template <int K> __device__ __forceinline__ void dmaT(const Ctx& c, unsigned m0a, unsigned soa) {
  constexpr int g = K >> 2, sub = K & 3;
  if constexpr (K == 15) m0a = c.tgrp == 3 * 4096u ? 130048u - 52224u : m0a;
  dma1<g * 16384u, g * 65536u, sub * 1024>(m0a, c.vT[sub], c.srdT, soa);
}

template <int J, bool LAST>
__device__ __forceinline__ void step(Ctx& c, unsigned wq, unsigned lb, unsigned tq, unsigned& slot) {
  constexpr int cur = setOf(J), JN = (J + 1) % 13, nxt = setOf(JN);
  constexpr bool hasNext = !(LAST && J == 12);
  constexpr int ns = nS(J, LAST), k0 = firstS(J);
  constexpr bool doW = nB(J, LAST) != 0;
  constexpr unsigned WO = (unsigned)((J + 4) % 13) * 131072u + ((J + 4) >= 13 ? 32768u : 0u);
  const unsigned slotN = (slot + 2048) & 8191;
  if constexpr (J == 12 && !LAST) { waitV<4>(); waitL<0>(); bar(); }
  else { if constexpr (hasNext) waitV<opsAll(J - 2, LAST) + opsAll(J - 1, LAST)>(); waitL<3>(); }
  const unsigned t0 = c.tb0 + slotN, t1 = c.tb1 + slotN, ms = c.ring + slot;
  mfma<regC(0, 0), regW(cur, 0), regE(cur, 0)>();
  if constexpr (hasNext) readW<regW(nxt, 0), 0>(t0);
  mfma<regC(0, 1), regW(cur, 1), regE(cur, 0)>();
  if constexpr (hasNext) readW<regW(nxt, 1), 0>(t1);
  if constexpr (hasNext) waitL<6>(); else waitL<2>();
  mfma<regC(1, 0), regW(cur, 0), regE(cur, 1)>();
  if constexpr (hasNext) readE<regE(nxt, 0)>(c.addr[JN][0]);
  if constexpr (ns >= 1) dmaT<k0>(c, lb, tq);
  mfma<regC(1, 1), regW(cur, 1), regE(cur, 1)>();
  if constexpr (doW) dma1<0, WO, 0>(ms, c.vW0, c.srdW, wq);
  if constexpr (hasNext) waitL<6>(); else waitL<1>();
  mfma<regC(2, 0), regW(cur, 0), regE(cur, 2)>();
  if constexpr (hasNext) readE<regE(nxt, 1)>(c.addr[JN][1]);
  if constexpr (ns >= 2) dmaT<k0 + 1>(c, lb, tq);
  mfma<regC(2, 1), regW(cur, 1), regE(cur, 2)>();
  if constexpr (doW) dma1<0, WO, 1024>(ms, c.vW1, c.srdW, wq);
  if constexpr (hasNext) waitL<6>(); else waitL<0>();
  mfma<regC(3, 0), regW(cur, 0), regE(cur, 3)>();
  if constexpr (hasNext) readE<regE(nxt, 2)>(c.addr[JN][2]);
  mfma<regC(3, 1), regW(cur, 1), regE(cur, 3)>();
  if constexpr (hasNext) readE<regE(nxt, 3)>(c.addr[JN][3]);
  slot = slotN;
}

template <bool LAST> __device__ __forceinline__ void phase(Ctx& c, int q, unsigned& slot) {
  const unsigned wq = c.wbase + (unsigned)q * 32768u, lb = (unsigned)(((q + 1) & 1) << 16) + c.tgrp,
                 tq = c.tgrp * 4u + (unsigned)(q + 1) * 64u;
  step<0, LAST>(c, wq, lb, tq, slot); step<1, LAST>(c, wq, lb, tq, slot); step<2, LAST>(c, wq, lb, tq, slot); step<3, LAST>(c, wq, lb, tq, slot);
  step<4, LAST>(c, wq, lb, tq, slot); step<5, LAST>(c, wq, lb, tq, slot); step<6, LAST>(c, wq, lb, tq, slot); step<7, LAST>(c, wq, lb, tq, slot);
  step<8, LAST>(c, wq, lb, tq, slot); step<9, LAST>(c, wq, lb, tq, slot); step<10, LAST>(c, wq, lb, tq, slot); step<11, LAST>(c, wq, lb, tq, slot);
  step<12, LAST>(c, wq, lb, tq, slot);
}

template <int R> __device__ __forceinline__ void zero1() { asm volatile("v_accvgpr_write_b32 a[%c0], 0" ::"i"(R)); }
template <int R, int N> struct Zero { static __device__ __forceinline__ void run() { zero1<R>(); Zero<R + 1, N - 1>::run(); } };
template <int R> struct Zero<R, 0> { static __device__ __forceinline__ void run() {} };

template <int R, int OFF> __device__ __forceinline__ void writeAcc(unsigned a) {
  asm volatile("ds_write_b128 %0, a[%c1:%c2] offset:%c3" ::"v"(a), "i"(R), "i"(R + 3), "i"(OFF) : "memory");
}
template <int R> __device__ __forceinline__ float readAcc() {
  float v; asm volatile("v_accvgpr_read_b32 %0, a[%c1]" : "=v"(v) : "i"(R)); return v;
}
template <int G> __device__ __forceinline__ f32x4 readAcc4() {
  f32x4 v; v.x = readAcc<G * 4>(); v.y = readAcc<G * 4 + 1>(); v.z = readAcc<G * 4 + 2>(); v.w = readAcc<G * 4 + 3>(); return v;
}

__device__ __forceinline__ float sumX8(float x) {
  return x + __int_as_float(__builtin_amdgcn_update_dpp(0, __float_as_int(x), 0x128, 0xf, 0xf, true));
}
__device__ __forceinline__ float sumX16(float x) {
  const auto r = __builtin_amdgcn_permlane16_swap(__float_as_uint(x), __float_as_uint(x), false, false);
  return __uint_as_float(r[0]) + __uint_as_float(r[1]);
}
__device__ __forceinline__ float sumX32(float x) {
  const auto r = __builtin_amdgcn_permlane32_swap(__float_as_uint(x), __float_as_uint(x), false, false);
  return __uint_as_float(r[0]) + __uint_as_float(r[1]);
}
__device__ __forceinline__ float rbf(float x) { return __bfloat162float(__float2bfloat16(x)); }

__global__ __launch_bounds__(256, 1) void wide_deep_kernel(
    const int* __restrict__ idx, const unsigned short* __restrict__ table, const unsigned short* __restrict__ xw,
    const unsigned short* __restrict__ ww, const unsigned short* __restrict__ bw, const unsigned short* __restrict__ W1,
    const unsigned short* __restrict__ b1, const unsigned short* __restrict__ W2, const unsigned short* __restrict__ b2,
    __hip_bfloat16* __restrict__ out, unsigned char* ws) {
  extern __shared__ __attribute__((aligned(16))) unsigned char lds[];
  const int tid = threadIdx.x, lane = tid & 63;
  const int w = __builtin_amdgcn_readfirstlane(tid >> 6);
  const int nt = blockIdx.x & 7, mt = blockIdx.x >> 3;
  const int n0 = nt * 64, m0 = mt * 128;
  const int fh = w >> 1, ks = w & 1;

  asm volatile("" ::"s"(idx), "s"(table), "s"(xw), "s"(ww), "s"(bw), "s"(W1), "s"(b1), "s"(W2), "s"(b2), "s"(out), "s"(ws));
  unsigned warm = 0, warm2 = 0;
  {
    const int e3 = mt * 94 + ((tid >> 1) < 94 ? (tid >> 1) : 93), ln = e3 < 3000 ? e3 : 2999;
    const unsigned short* p2 = table + (size_t)(ln / 3) * D_ + (1 + ln % 3) * 32;
    const int t = tid < 208 ? tid : 207;
    const unsigned short* p = W1 + (size_t)(mt * 104 + (t >> 1)) * H_ + n0 + (t & 1) * 32;
    asm volatile("global_load_dword %0, %2, off\n\tglobal_load_dword %1, %3, off"
                 : "=&v"(warm2), "=&v"(warm) : "v"(p2), "v"(p) : "memory");
  }
  unsigned epoch, wv_[2] = {0, 0}, bwv = 0, b2v = 0, warm3 = 0;
#define LDH(dst, ptr) asm volatile("global_load_ushort %0, %1, off" : "=v"(dst) : "v"(ptr) : "memory")
  asm volatile("global_load_dword %0, %1, off sc1" : "=v"(epoch) : "v"(ws + mt * 128) : "memory");
  if (w == 0) {
    const int g = lane >> 3, g2 = g + 8 < ND_ ? g + 8 : ND_ - 1;
    LDH(wv_[0], ww + g); LDH(wv_[1], ww + g2); LDH(bwv, bw); LDH(b2v, b2);
    LDH(warm3, out + m0 + nt * 16);
  }
#undef LDH
  Ctx c;
  const __amdgpu_buffer_rsrc_t srdI = __builtin_amdgcn_make_buffer_rsrc((void*)idx, (short)0, B_ * F_ * 4, 0x00020000);
  {
    const unsigned vI = (unsigned)lane * 16u, p3 = w == 0 ? 12u : (unsigned)w + 8u;
    dma1<65536u, 0, 0>((unsigned)w * 1024u, vI, srdI, (unsigned)(m0 * F_ * 4 + w * 1024));
    dma1<65536u + 4096u, 4096u, 0>((unsigned)w * 1024u, vI, srdI, (unsigned)(m0 * F_ * 4 + w * 1024));
    dma1<65536u, 0, 0>(p3 * 1024u, vI, srdI, (unsigned)(m0 * F_ * 4) + p3 * 1024u);
    dma1<65536u, 0, 0>((w == 0 ? 8u : 12u) * 1024u, vI, srdI, (unsigned)(m0 * F_ * 4) + (w == 0 ? 8u : 12u) * 1024u);
  }
  {
    const unsigned char* src = lane >= 16 && lane < 42 ? (const unsigned char*)(xw + (size_t)(m0 + nt * 16) * ND_) + (lane - 16) * 16
                               : (const unsigned char*)((lane >> 3) == 1 ? W2 : b1) + n0 * 2 + (lane & 7) * 16;
    asm volatile("s_mov_b32 m0, %0\n\ts_nop 0\n\tglobal_load_lds_dwordx4 %1, off" ::"s"(SMALL0), "v"(src) : "memory");
  }
  c.srdW = __builtin_amdgcn_make_buffer_rsrc((void*)W1, (short)0, WREC, 0x00020000);
  c.srdT = __builtin_amdgcn_make_buffer_rsrc((void*)table, (short)0, TREC, 0x00020000);
  c.ring = RING0 + w * 8192;
  c.wbase = (unsigned)(fh * 13 * 128 + ks * 16) * 1024u;
  c.tgrp = (unsigned)w * 4096u;
  c.vT[0] = (unsigned)(lane >> 2) * 256u + (unsigned)(((lane & 3) ^ ((lane >> 4) & 3)) * 16);
  c.vT[1] = c.vT[0] + 3072u; c.vT[2] = c.vT[0] + 6144u; c.vT[3] = c.vT[0] + 9216u;
  c.vW0 = (unsigned)(lane >> 3) * 1024u + (unsigned)(((lane & 7) ^ (((lane >> 4) & 1) << 2)) * 16) + (unsigned)n0 * 2u;
  c.vW1 = c.vW0 + 7168u;
  {
    const int qq = (lane & 15) >> 2, p = lane & 3, swz = (qq >> 1) & 1;
    const unsigned base = c.ring + (unsigned)((8 * (lane >> 5) + qq) * 128 + ((lane >> 4) & 1) * 32 + p * 8);
    c.tb0 = base + (unsigned)(swz * 64);
    c.tb1 = base + (unsigned)((swz ^ 1) * 64);
  }
  asm volatile("" ::: ACLOB);
  const unsigned tq0 = c.tgrp * 4u;
  dmaT<0>(c, c.tgrp, tq0); dmaT<1>(c, c.tgrp, tq0); Zero<0, 16>::run();
  dma1<0, 0, 0>(c.ring, c.vW0, c.srdW, c.wbase); dma1<0, 0, 1024>(c.ring, c.vW1, c.srdW, c.wbase); Zero<16, 16>::run();
  dmaT<2>(c, c.tgrp, tq0); dmaT<3>(c, c.tgrp, tq0); Zero<32, 16>::run();
  dmaT<4>(c, c.tgrp, tq0); dmaT<5>(c, c.tgrp, tq0); Zero<48, 16>::run();
  dmaT<6>(c, c.tgrp, tq0); dmaT<7>(c, c.tgrp, tq0); Zero<64, 16>::run();
  waitV<11>();
  bar();
  {
    const unsigned cs = (unsigned)(ks * 2 + (lane >> 5)) << 4;
#define ADDR_BLOCK(i)                                                                          \
    {                                                                                          \
      const int row = ((w + i) & 3) * 32 + (lane & 31);                                        \
      const int* ip = (const int*)(lds + 65536 + row * (F_ * 4) + fh * 52);                    \
      unsigned v[13];                                                                          \
      _Pragma("unroll") for (int j = 0; j < 13; ++j) v[j] = (unsigned)ip[j];                   \
      dmaT<8 + 2 * i>(c, c.tgrp, tq0); dmaT<9 + 2 * i>(c, c.tgrp, tq0);                  \
      Zero<80 + 12 * i, 12>::run();                                                            \
      _Pragma("unroll") for (int j = 0; j < 13; ++j)                                           \
        c.addr[j][i] = (v[j] << 6) | (((v[j] << 2) & 0x30u) ^ cs);                             \
    }
    ADDR_BLOCK(0) ADDR_BLOCK(1) ADDR_BLOCK(2) ADDR_BLOCK(3)
#undef ADDR_BLOCK
  }
  dma1<2048, 131072, 0>(c.ring, c.vW0, c.srdW, c.wbase); dma1<2048, 131072, 1024>(c.ring, c.vW1, c.srdW, c.wbase);
  dma1<4096, 2 * 131072, 0>(c.ring, c.vW0, c.srdW, c.wbase); dma1<4096, 2 * 131072, 1024>(c.ring, c.vW1, c.srdW, c.wbase);
  dma1<6144, 3 * 131072, 0>(c.ring, c.vW0, c.srdW, c.wbase); dma1<6144, 3 * 131072, 1024>(c.ring, c.vW1, c.srdW, c.wbase);
  waitL<0>();
  waitV<2 + 4>();
  asm volatile("" ::"v"(warm), "v"(warm2), "v"(warm3));
  bar();
  readW<regW(2, 0), 0>(c.tb0);
  readW<regW(2, 1), 0>(c.tb1);
  readE<regE(2, 0)>(c.addr[0][0]); readE<regE(2, 1)>(c.addr[0][1]);
  readE<regE(2, 2)>(c.addr[0][2]); readE<regE(2, 3)>(c.addr[0][3]);

  unsigned slot = 0;
#pragma nounroll
  for (int q = 0; q < 3; ++q) {
    phase<false>(c, q, slot);
  }
  u32x2 b1v[8], w2v[8];
  unsigned xv[4];
  {
    const unsigned char* sp = lds + SMALL0 + (lane >> 5) * 8;
#pragma unroll
    for (int g = 0; g < 8; ++g) { b1v[g] = *(const u32x2*)(sp + g * 16); w2v[g] = *(const u32x2*)(sp + 128 + g * 16); }
    const int g = lane >> 3, g2 = g + 8 < ND_ ? g + 8 : ND_ - 1;
    const unsigned short* xp = (const unsigned short*)(lds + SMALL0 + 256) + (lane & 7) * 2 * ND_;
    xv[0] = xp[g]; xv[1] = xp[ND_ + g]; xv[2] = xp[g2]; xv[3] = xp[ND_ + g2];
  }
  phase<true>(c, 3, slot);

#pragma unroll
  for (int g = 0; g < 8; ++g) asm volatile("" : "+v"(b1v[g]), "+v"(w2v[g]));
  asm volatile("s_nop 7\n\ts_waitcnt vmcnt(0) lgkmcnt(0)\n\ts_barrier"
               : "+v"(xv[0]), "+v"(xv[1]), "+v"(xv[2]), "+v"(xv[3]), "+v"(wv_[0]), "+v"(wv_[1]), "+v"(bwv), "+v"(b2v), "+v"(epoch)
               :: "memory");
  f32x4 h[8], w2f[8];
  const unsigned s1 = (unsigned)(RED0 + (((w + 1) & 3) * 3 + 0) * 8192 + lane * 16);
  const unsigned s2 = (unsigned)(RED0 + (((w + 2) & 3) * 3 + 1) * 8192 + lane * 16);
  const unsigned s3 = (unsigned)(RED0 + (((w + 3) & 3) * 3 + 2) * 8192 + lane * 16);
#define SEND(g)                                                                                                   \
  writeAcc<32 + g * 4, g * 1024>(s1);                                                                             \
  h[g] = readAcc4<g>();                                                                                           \
  h[g].x += __uint_as_float(b1v[g].x << 16); h[g].y += __uint_as_float(b1v[g].x & 0xffff0000u);                   \
  asm volatile("" : "+v"(h[g]));                                                                                  \
  writeAcc<64 + g * 4, g * 1024>(s2);                                                                             \
  h[g].z += __uint_as_float(b1v[g].y << 16); h[g].w += __uint_as_float(b1v[g].y & 0xffff0000u);                   \
  w2f[g].x = __uint_as_float(w2v[g].x << 16); w2f[g].y = __uint_as_float(w2v[g].x & 0xffff0000u);                 \
  asm volatile("" : "+v"(h[g]), "+v"(w2f[g].x), "+v"(w2f[g].y));                                                  \
  writeAcc<96 + g * 4, g * 1024>(s3);                                                                             \
  w2f[g].z = __uint_as_float(w2v[g].y << 16); w2f[g].w = __uint_as_float(w2v[g].y & 0xffff0000u);                 \
  asm volatile("" : "+v"(w2f[g].z), "+v"(w2f[g].w));
  SEND(0) SEND(1) SEND(2) SEND(3) SEND(4) SEND(5) SEND(6) SEND(7)
#undef SEND
  float wide = __uint_as_float(xv[0] << 16) * __uint_as_float(wv_[0] << 16), wide1 = __uint_as_float(xv[1] << 16) * __uint_as_float(wv_[0] << 16);
  if ((lane >> 3) + 8 < ND_) {
    wide += __uint_as_float(xv[2] << 16) * __uint_as_float(wv_[1] << 16);
    wide1 += __uint_as_float(xv[3] << 16) * __uint_as_float(wv_[1] << 16);
  }
  wide = sumX32(sumX16(sumX8(wide))); wide1 = sumX32(sumX16(sumX8(wide1)));
  asm volatile("s_waitcnt lgkmcnt(0)\n\ts_barrier" : "+v"(wide), "+v"(wide1) :: "memory");
  f32x4 r[24];
#pragma unroll
  for (int g = 0; g < 8; ++g) {
    const unsigned char* p = lds + RED0 + w * 3 * 8192 + g * 1024 + lane * 16;
    r[g * 3] = *(const f32x4*)p; r[g * 3 + 1] = *(const f32x4*)(p + 8192); r[g * 3 + 2] = *(const f32x4*)(p + 16384);
  }
  f32x4 pacc = {0.f, 0.f, 0.f, 0.f};
#pragma unroll
  for (int g = 0; g < 8; ++g) {
    if ((g & 1) == 0)
      asm volatile("" : "+v"(r[g * 3]), "+v"(r[g * 3 + 1]), "+v"(r[g * 3 + 2]), "+v"(r[g * 3 + 3]), "+v"(r[g * 3 + 4]), "+v"(r[g * 3 + 5]) :: "memory");
    const f32x4 s = (h[g] + r[g * 3]) + (r[g * 3 + 1] + r[g * 3 + 2]);
    pacc.x = fmaf(fmaxf(s.x, 0.f), w2f[g].x, pacc.x); pacc.y = fmaf(fmaxf(s.y, 0.f), w2f[g].y, pacc.y);
    pacc.z = fmaf(fmaxf(s.z, 0.f), w2f[g].z, pacc.z); pacc.w = fmaf(fmaxf(s.w, 0.f), w2f[g].w, pacc.w);
  }
  const float part = sumX32((pacc.x + pacc.y) + (pacc.z + pacc.w));

  const unsigned tag = epoch + 1u;
  gu64* gran = (gu64*)(ws + WS_GRAN);
  if (lane < 32)
    __hip_atomic_store(gran + (size_t)((mt * 8 + nt) * 128 + w * 32 + lane), ((unsigned long long)tag << 32) | __float_as_uint(part),
                       __ATOMIC_RELAXED, __HIP_MEMORY_SCOPE_AGENT);
  if (w == 0) {
    const unsigned char* gp = ws + WS_GRAN + (size_t)((mt * 8 + (lane >> 3)) * 128 + nt * 16 + (lane & 7) * 2) * 8;
    float d0, d1;
    {
      unsigned long long mk; unsigned n;
#define POLL(a, b, c, d)                                                                                      \
      "s_waitcnt vmcnt(2)\n\tv_cmp_eq_u32 vcc, %5, v" #b "\n\tv_cmp_eq_u32 %2, %5, v" #d "\n\t"           \
      "v_mov_b32 %0, v" #a "\n\tv_mov_b32 %1, v" #c "\n\ts_and_b64 vcc, vcc, %2\n\ts_cmp_eq_u64 vcc, exec\n\t" \
      "s_cbranch_scc1 .Lwd_got\n\tglobal_load_dwordx4 v[" #a ":" #d "], %4, off sc1\n\t"
      asm volatile("global_load_dwordx4 v[240:243], %4, off sc1\n\ts_sleep 5\n\t"
                   "global_load_dwordx4 v[244:247], %4, off sc1\n\ts_sleep 5\n\t"
                   "global_load_dwordx4 v[248:251], %4, off sc1\n\ts_mov_b32 %3, 0x200000\n"
                   ".Lwd_poll:\n\t"
                   POLL(240, 241, 242, 243) POLL(244, 245, 246, 247) POLL(248, 249, 250, 251)
                   "s_sub_u32 %3, %3, 1\n\ts_cmp_lg_u32 %3, 0\n\ts_cbranch_scc1 .Lwd_poll\n"
                   ".Lwd_got:"
                   : "=&v"(d0), "=&v"(d1), "=&s"(mk), "=&s"(n) : "v"(gp), "v"(tag)
                   : "v240", "v241", "v242", "v243", "v244", "v245", "v246", "v247", "v248", "v249", "v250", "v251",
                     "vcc", "scc", "memory");
#undef POLL
    }
    d0 = sumX32(sumX16(sumX8(d0))); d1 = sumX32(sumX16(sumX8(d1)));
    if (lane < 8) {
      const float bwf = __uint_as_float(bwv << 16), b2f = __uint_as_float(b2v << 16);
      const float z0 = rbf(rbf(rbf(wide) + bwf) + rbf(rbf(d0) + b2f)), z1 = rbf(rbf(rbf(wide1) + bwf) + rbf(rbf(d1) + b2f));
      __hip_bfloat162 o;
      o.x = __float2bfloat16(1.f / (1.f + __expf(-z0)));
      o.y = __float2bfloat16(1.f / (1.f + __expf(-z1)));
      *(__hip_bfloat162*)(out + m0 + nt * 16 + lane * 2) = o;
    }
    if (nt == 0 && lane == 0) __hip_atomic_store((gu32*)(ws + mt * 128), tag, __ATOMIC_RELAXED, __HIP_MEMORY_SCOPE_AGENT);
  }
}

extern "C" void kernel_launch(void* const* d_in, const int* in_sizes, int n_in, void* d_out, int out_size, void* d_ws,
                              size_t ws_size, hipStream_t stream) {
  (void)in_sizes; (void)n_in; (void)out_size;
  if (ws_size < (size_t)WS_GRAN + 32 * 8 * 128 * 8) return;
  (void)hipFuncSetAttribute((const void*)wide_deep_kernel, hipFuncAttributeMaxDynamicSharedMemorySize, LDS_BYTES);
  wide_deep_kernel<<<256, 256, LDS_BYTES, stream>>>(
      (const int*)d_in[0], (const unsigned short*)d_in[1], (const unsigned short*)d_in[2], (const unsigned short*)d_in[3],
      (const unsigned short*)d_in[4], (const unsigned short*)d_in[5], (const unsigned short*)d_in[6],
      (const unsigned short*)d_in[7], (const unsigned short*)d_in[8], (__hip_bfloat16*)d_out, (unsigned char*)d_ws);
}
```
